```python
import torch

def _softmax(x, axis=-1):
    return torch.softmax(x, dim=axis)
def _gelu(x):
    return torch.nn.functional.gelu(x, approximate="tanh")
def _silu(x):
    return torch.nn.functional.silu(x)
def _sigmoid(x):
    return torch.sigmoid(x)
def _layernorm(x, axis=-1, eps=1e-5):
    m = x.mean(dim=axis, keepdim=True)
    v = x.var(dim=axis, keepdim=True, unbiased=False)
    return (x - m) / torch.sqrt(v + eps)
def _scatter_add(tgt, idx, src, axis=0):
    return tgt.clone().index_add_(axis, idx, src)
def _segment_sum(src, seg, n):
    out = torch.zeros((n,) + src.shape[1:], dtype=src.dtype)
    return out.index_add_(0, seg, src)
def _pad(x, pw):
    flat = tuple(v for pair in reversed(pw) for v in pair)
    return torch.nn.functional.pad(x, flat)

kernel_name = 'gated_mix__ssm_conv_s4__geglu_ffn__prompt'

def setup_inputs():

    _w = (lambda a: (lambda t: t.to(torch.bfloat16) if t.is_floating_point() else t)(torch.as_tensor(a)))
    def _A_setup():
        B, L, D, N = 1, 2048, 512, 128
        A_re = -(lambda _x: torch.exp(torch.as_tensor(_x)))((lambda *s: torch.randn(*s, dtype=torch.bfloat16) * 6.4)(D, N))
        A_im = torch.pi * (lambda *s: torch.randn(*s, dtype=torch.bfloat16) * 0.05)(D, N)
        B_re = (lambda *s: torch.randn(*s, dtype=torch.bfloat16) * 0.1)(D, N)
        B_im = (lambda *s: torch.randn(*s, dtype=torch.bfloat16) * 0.1)(D, N)
        C_re = (lambda *s: torch.randn(*s, dtype=torch.bfloat16) * 0.1)(D, N)
        C_im = (lambda *s: torch.randn(*s, dtype=torch.bfloat16) * 0.1)(D, N)
        log_dt = (lambda *s: torch.randn(*s, dtype=torch.bfloat16) * 0.05)(D) * 0.5 - 4.0
        Dskip = (lambda *s: torch.randn(*s, dtype=torch.bfloat16) * 0.05)(D)
        return {
            "u": _w((lambda *s: torch.randn(*s, dtype=torch.bfloat16) * 0.05)(B, L, D)),
            "A_re": _w(A_re),
            "A_im": _w(A_im),
            "B_re": _w(B_re),
            "B_im": _w(B_im),
            "C_re": _w(C_re),
            "C_im": _w(C_im),
            "log_dt": _w(log_dt),
            "Dskip": _w(Dskip),
        }
    _A_in = _A_setup()

    def _B_setup():
        B, S, D, F = 1, 2048, 512, 1376
        return {
            "x": _w((lambda *s: torch.randn(*s, dtype=torch.bfloat16) * 0.05)(B, S, D)),
            "w_gate": _w((lambda *s: torch.randn(*s, dtype=torch.bfloat16) * 0.05)(D, F)),
            "w_up": _w((lambda *s: torch.randn(*s, dtype=torch.bfloat16) * 0.05)(D, F)),
            "w_down": _w((lambda *s: torch.randn(*s, dtype=torch.bfloat16) * 0.05)(F, D)),
        }
    _B_in = _B_setup()

    return {
        "A_u": _A_in["u"],
        "A_A_re": _A_in["A_re"],
        "A_A_im": _A_in["A_im"],
        "A_B_re": _A_in["B_re"],
        "A_B_im": _A_in["B_im"],
        "A_C_re": _A_in["C_re"],
        "A_C_im": _A_in["C_im"],
        "A_log_dt": _A_in["log_dt"],
        "A_Dskip": _A_in["Dskip"],
        "B_x": _B_in["x"],
        "B_w_gate": _B_in["w_gate"],
        "B_w_up": _B_in["w_up"],
        "B_w_down": _B_in["w_down"],
        "gate_w": _w((lambda *s: torch.randn(*s, dtype=torch.bfloat16) * 0.05)(512) * 16.0),
    }

def reference(A_u, A_A_re, A_A_im, A_B_re, A_B_im, A_C_re, A_C_im, A_log_dt, A_Dskip, B_x, B_w_gate, B_w_up, B_w_down, gate_w):
    def _A_ref(u, A_re, A_im, B_re, B_im, C_re, C_im, log_dt, Dskip):
        Bsz, L, D = u.shape
        dt = torch.exp(log_dt)[:, None]
        A = A_re + 1j * A_im
        Bc = B_re + 1j * B_im
        Cc = C_re + 1j * C_im
        logdA = dt * A
        dB = dt * Bc
        l = (lambda x, dt: x.to(dt))(torch.arange(L), A_re.dtype)
        P = torch.exp(logdA[:, :, None] * l[None, None, :])
        K = torch.einsum("dn,dnl,dn->dl", Cc, P, dB).real
        L2 = 2 * L
        Kp = _pad(K, ((0, 0), (0, L)))
        up = (lambda x, *d: x.permute(*d))(u, 0, 2, 1)
        up = _pad(up, ((0, 0), (0, 0), (0, L)))
        Kf = (lambda x, axes=(-2,-1): torch.fft.rfft2(x.float(), dim=axes))(Kp[:, None, :], axes=(-2, -1))
        Uf = (lambda x, axes=(-2,-1): torch.fft.rfft2(x.float(), dim=axes))(up[:, :, None, :], axes=(-2, -1))
        Yf = Uf * Kf[None, :, :, :]
        y = (lambda x, s, axes=(-2,-1): torch.fft.irfft2(x, s=s, dim=axes).to(torch.bfloat16))(Yf, (1, L2), axes=(-2, -1))[:, :, 0, :L]
        y = (lambda x, *d: x.permute(*d))(y, 0, 2, 1) + u * Dskip[None, None, :]
        return y

    def _B_ref(x, w_gate, w_up, w_down):
        return (_gelu(x @ w_gate) * (x @ w_up)) @ w_down

    x = A_u
    g = _sigmoid((x * gate_w).sum(-1, keepdims=True))
    a = _A_ref(x, A_A_re, A_A_im, A_B_re, A_B_im, A_C_re, A_C_im, A_log_dt, A_Dskip)
    b = _B_ref(x, B_w_gate, B_w_up, B_w_down)
    return g * a + (1 - g) * b
```

```cpp
#include <hip/hip_runtime.h>
#include <hip/hip_bf16.h>
#include <stdint.h>

#define LAS __attribute__((address_space(3)))
#define GAS __attribute__((address_space(1)))
typedef short  v4s  __attribute__((ext_vector_type(4)));
typedef short  v8s  __attribute__((ext_vector_type(8)));
typedef __bf16 v8bf __attribute__((ext_vector_type(8)));
typedef __bf16 v2bf __attribute__((ext_vector_type(2)));
typedef float  v2f  __attribute__((ext_vector_type(2)));
typedef float  v4f  __attribute__((ext_vector_type(4)));
typedef float  v16f __attribute__((ext_vector_type(16)));
typedef unsigned v4u __attribute__((ext_vector_type(4)));
typedef unsigned v2u __attribute__((ext_vector_type(2)));
typedef unsigned short u16;

constexpr int L_ = 2048, D_ = 512, N_ = 128, F_ = 1376;
constexpr int NG = F_ / 16;

constexpr size_t WS_H = 0;
constexpr size_t WS_A = WS_H + (size_t)64 * NG * 1024;
constexpr size_t WS_G = WS_A + (size_t)256 * 2048 * 4;

__device__ __forceinline__ float bf2f(u16 v) { return __uint_as_float((unsigned)v << 16); }
__device__ __forceinline__ float bflo(unsigned v) { return __uint_as_float(v << 16); }
__device__ __forceinline__ float bfhi(unsigned v) { return __uint_as_float(v & 0xffff0000u); }
__device__ __forceinline__ unsigned pk2(float lo, float hi) {
    v2f f = {lo, hi};
    return __builtin_bit_cast(unsigned, __builtin_convertvector(f, v2bf));
}
__device__ __forceinline__ float rbf(float x) { return bflo(pk2(x, x)); }

#define WAIT_VM(n)  asm volatile("s_waitcnt vmcnt(" #n ")" ::: "memory")
#define WAIT_LGKM0() asm volatile("s_waitcnt lgkmcnt(0)" ::: "memory")
#define BAR() __builtin_amdgcn_s_barrier()

typedef v4u rsrc_t;
__device__ __forceinline__ rsrc_t mkrsrc(const void* p, unsigned bytes) {
    uint64_t a = (uint64_t)p;
    rsrc_t r = {(unsigned)a, (unsigned)(a >> 32) & 0xffffu, bytes, 0x00020000u};
    return r;
}
__device__ __forceinline__ unsigned lds_addr(const char* p) { return (unsigned)(uintptr_t)(LAS const char*)p; }
__device__ __forceinline__ void glds16(rsrc_t r, char* lds_dst, unsigned voff, unsigned soff) {
    asm volatile("s_mov_b32 m0, %0\n\ts_nop 0\n\tbuffer_load_dwordx4 %1, %2, %3 offen lds"
                 :: "s"(lds_addr(lds_dst)), "v"(voff), "s"(r), "s"(soff) : "memory");
}
__device__ __forceinline__ v8bf tr_frag(const char* p0, const char* p1) {
    v4s a = __builtin_amdgcn_ds_read_tr16_b64_v4i16((LAS v4s*)p0);
    v4s b = __builtin_amdgcn_ds_read_tr16_b64_v4i16((LAS v4s*)p1);
    v8s r = __builtin_shufflevector(a, b, 0, 1, 2, 3, 4, 5, 6, 7);
    return __builtin_bit_cast(v8bf, r);
}

struct cf { float r, i; };
__device__ __forceinline__ cf cmul(cf a, cf b) { return {a.r * b.r - a.i * b.i, a.r * b.i + a.i * b.r}; }
__device__ __forceinline__ cf cexp_(float al, float be, float e) {
    float m = __builtin_amdgcn_exp2f(al * e);
    float th = __builtin_amdgcn_fractf(be * e);
    return {m * __builtin_amdgcn_cosf(th), m * __builtin_amdgcn_sinf(th)};
}

constexpr int TP = 272;
constexpr int S_T = 0;
constexpr int S_TW = 53 * TP;
constexpr int S_KC = 0;
constexpr int KCP = 4160;
constexpr int S_Y = 0;
constexpr int S_KD = 4 * S_TW;
constexpr int KDN = 704;
constexpr int S_U = S_KD + 4 * KDN * 4;
constexpr int UP = 2288 * 2;
constexpr int S_END = S_U + 2 * UP;

__global__ __launch_bounds__(256) void k_ssm(const u16* __restrict__ u, const u16* __restrict__ Are, const u16* __restrict__ Aim,
        const u16* __restrict__ Bre, const u16* __restrict__ Bim, const u16* __restrict__ Cre, const u16* __restrict__ Cim,
        const u16* __restrict__ logdt, const u16* __restrict__ Dskip, const u16* __restrict__ gw,
        unsigned* __restrict__ a2, float* __restrict__ gout) {
    extern __shared__ __attribute__((aligned(16))) char lds[];
    const int t = threadIdx.x, lane = t & 63, w = __builtin_amdgcn_readfirstlane(t >> 6);
    const int b = blockIdx.x, ch = w >> 1, hf = w & 1;
    const int d = 2 * b + ch;
    unsigned uu[8];
#pragma unroll
    for (int i = 0; i < 8; ++i) uu[i] = *(const unsigned*)(u + (size_t)(t * 8 + i) * D_ + 2 * b);
    {
        const int row = 8 * b + 2 * w + (lane >> 5), i = lane & 31;
        const v4u x0 = *(const v4u*)(u + (size_t)row * D_ + 16 * i), x1 = *(const v4u*)(u + (size_t)row * D_ + 16 * i + 8);
        const v4u g0 = *(const v4u*)(gw + 16 * i), g1 = *(const v4u*)(gw + 16 * i + 8);
        float s = 0.f;
#pragma unroll
        for (int k = 0; k < 4; ++k) {
            unsigned p0 = pk2(bflo(x0[k]) * bflo(g0[k]), bfhi(x0[k]) * bfhi(g0[k]));
            unsigned p1 = pk2(bflo(x1[k]) * bflo(g1[k]), bfhi(x1[k]) * bfhi(g1[k]));
            s += bflo(p0) + bfhi(p0) + bflo(p1) + bfhi(p1);
        }
#pragma unroll
        for (int m = 16; m >= 1; m >>= 1) s += __shfl_xor(s, m);
        s = rbf(s);
        float g = rbf(1.f / (1.f + __expf(-s)));
        if (i == 0) gout[row] = g;
    }
    const int n = 64 * hf + lane, pi = d * N_ + n;
    const float dt = rbf(__expf(bf2f(logdt[d])));
    const float al = dt * bf2f(Are[pi]) * 1.44269504088896341f, be = dt * bf2f(Aim[pi]) * 0.15915494309189535f;
    const cf wv = cmul(cf{bf2f(Cre[pi]), bf2f(Cim[pi])}, cf{dt * bf2f(Bre[pi]), dt * bf2f(Bim[pi])});
    char* T = lds + S_T + w * S_TW + lane * 4;
    {
        const cf z1 = cexp_(al, be, 1.f), z8 = cexp_(al, be, 8.f), z256 = cexp_(al, be, 256.f);
        const cf z16 = cmul(z8, z8);
        cf e = {1.f, 0.f}, o = z8;
#pragma unroll
        for (int r = 0; r < 32; r += 2) {
            *(unsigned*)(T + r * TP) = pk2(e.r, e.i);
            *(unsigned*)(T + (r + 1) * TP) = pk2(o.r, o.i);
            if (r + 2 < 32) { e = cmul(e, z16); o = cmul(o, z16); }
        }
        char* T2 = T + 32 * TP;
        cf p = wv;
#pragma unroll
        for (int c = 0; c < 8; ++c) { *(unsigned*)(T2 + c * TP) = pk2(p.r, -p.i); if (c < 7) p = cmul(p, z1); }
        const cf z2 = cmul(z1, z1), z4 = cmul(z2, z2);
        cf q = cmul(wv, z256);
        cf q2 = q;
#pragma unroll
        for (int c = 0; c < 4; ++c) { *(unsigned*)(T2 + (8 + c) * TP) = pk2(q2.r, -q2.i); if (c < 3) q2 = cmul(q2, z2); }
#pragma unroll
        for (int c = 0; c < 2; ++c) {
            q = cmul(q, z256);
            cf q4 = cmul(q, z4);
            *(unsigned*)(T2 + (12 + 2 * c) * TP) = pk2(q.r, -q.i);
            *(unsigned*)(T2 + (13 + 2 * c) * TP) = pk2(q4.r, -q4.i);
        }
#pragma unroll
        for (int c = 0; c < 5; ++c) { q = cmul(q, z256); *(unsigned*)(T2 + (16 + c) * TP) = pk2(q.r, -q.i); }
    }
    {
        v4u p0, p1;
#pragma unroll
        for (int i = 0; i < 4; ++i) {
            p0[i] = (uu[2 * i] & 0xffffu) | (uu[2 * i + 1] << 16);
            p1[i] = (uu[2 * i] >> 16) | (uu[2 * i + 1] & 0xffff0000u);
        }
        *(v4u*)(lds + S_U + (240 + 8 * t) * 2) = p0;
        *(v4u*)(lds + S_U + UP + (240 + 8 * t) * 2) = p1;
        if (t < 60) *(v4u*)(lds + S_U + (t >= 30 ? UP : 0) + (t % 30) * 16) = (v4u){0, 0, 0, 0};
    }
    v16f kacc = {};
    {
        const int r = lane & 31, hh = lane >> 5;
        const char* TA = lds + S_T + w * S_TW + r * TP + hh * 16;
        const char* TB = lds + S_T + w * S_TW + (32 + (r < 20 ? r : 20)) * TP + hh * 16;
#pragma unroll
        for (int s = 0; s < 8; ++s) {
            v8bf fa = *(const v8bf*)(TA + s * 32), fb = *(const v8bf*)(TB + s * 32);
            kacc = __builtin_amdgcn_mfma_f32_32x32x16_bf16(fa, fb, kacc, 0, 0, 0);
        }
        const int c = r;
        int base, stride;
        if (c < 8) { base = c; stride = 8; }
        else if (c < 12) { base = 256 + (c - 8); stride = 4; }
        else if (c < 16) { base = 384 + 64 * ((c - 12) >> 1) + ((c - 12) & 1); stride = 2; }
        else if (c <= 20) { base = 512 + 32 * (c - 16); stride = 1; }
        else { base = 672; stride = 1; }
        float* KD = (float*)(lds + S_KD) + w * KDN;
#pragma unroll
        for (int q = 0; q < 16; ++q) KD[base + stride * ((q & 3) + 8 * (q >> 2) + 4 * hh)] = kacc[q];
    }
    __syncthreads();
#pragma unroll
    for (int c = 0; c < 2; ++c) {
        const float* K0 = (const float*)(lds + S_KD) + (2 * c) * KDN;
        const float* K1 = K0 + KDN;
        float v[12];
#pragma unroll
        for (int k = 0; k < 12; ++k) {
            int j = 2047 - (8 * t + k);
            int jj = j < 0 ? 0 : j;
            int idx = jj < 128 ? jj : (int)(pk2((float)jj, 0.f) & 0xffffu) - 17024;
            float x = K0[idx] + K1[idx];
            v[k] = j < 0 ? 0.f : x;
        }
#pragma unroll
        for (int s = 0; s < 4; ++s) {
            v4u o = {pk2(v[s], v[s + 1]), pk2(v[s + 2], v[s + 3]), pk2(v[s + 4], v[s + 5]), pk2(v[s + 6], v[s + 7])};
            *(v4u*)(lds + S_KC + (c * 4 + s) * KCP + t * 16) = o;
        }
        if (t < 8) *(v4u*)(lds + S_KC + (c * 4 + (t >> 1)) * KCP + 4096 + (t & 1) * 16) = (v4u){0, 0, 0, 0};
    }
    __syncthreads();
    v4f y[8];
#pragma unroll
    for (int p = 0; p < 8; ++p) y[p] = (v4f){0, 0, 0, 0};
    {
        const int r = lane & 15, g = lane >> 4, rp = 15 - r;
        const char* KA = lds + S_KC + (ch * 4 + (rp & 3)) * KCP + (8 * g + (rp & 12) + 128 * hf) * 2;
        const char* UB = lds + S_U + ch * UP + (16 * r + 8 * g + 128 * hf) * 2;
#pragma unroll 1
        for (int tt = 0; tt < 4; ++tt) {
            v8bf fa[8], fb[8];
#pragma unroll
            for (int m = 0; m < 8; ++m) {
                v2u lo = *(const v2u*)(KA + tt * 64 + (7 - m) * 512), hi = *(const v2u*)(KA + tt * 64 + (7 - m) * 512 + 8);
                v4u x = {lo[0], lo[1], hi[0], hi[1]};
                fa[m] = __builtin_bit_cast(v8bf, x);
                fb[m] = *(const v8bf*)(UB + tt * 64 + m * 512);
            }
#pragma unroll
            for (int m = 0; m < 8; ++m)
#pragma unroll
                for (int q = 0; q + m < 8; ++q)
                    y[m + q] = __builtin_amdgcn_mfma_f32_16x16x32_bf16(fa[m], fb[q], y[m + q], 0, 0, 0);
        }
    }
    __syncthreads();
#pragma unroll
    for (int p = 0; p < 8; ++p) *(v4f*)(lds + S_Y + w * 8192 + (p * 64 + lane) * 16) = y[p];
    __syncthreads();
    {
        const int p = t >> 5, l1 = (t >> 1) & 15, g0 = 2 * (t & 1);
        const float ds0 = bf2f(Dskip[2 * b]), ds1 = bf2f(Dskip[2 * b + 1]);
        v4u o[2];
#pragma unroll
        for (int h = 0; h < 2; ++h) {
            const char* yp = lds + S_Y + (p * 64 + (g0 + h) * 16 + l1) * 16;
            v4f y0 = *(const v4f*)(yp) + *(const v4f*)(yp + 8192);
            v4f y1 = *(const v4f*)(yp + 16384) + *(const v4f*)(yp + 24576);
#pragma unroll
            for (int k = 0; k < 4; ++k) {
                unsigned yb = pk2(y0[k], y1[k]);
                unsigned sk = pk2(bflo(uu[4 * h + k]) * ds0, bfhi(uu[4 * h + k]) * ds1);
                o[h][k] = pk2(bflo(yb) + bflo(sk), bfhi(yb) + bfhi(sk));
            }
        }
        v4u* dst = (v4u*)(a2 + (size_t)b * L_ + 8 * t);
        dst[0] = o[0]; dst[1] = o[1];
    }
}

constexpr int G1_X = 0, G1_WG = 16384, G1_WU = G1_WG + 12288 + 32, G1_STAGE = 40960 + 64;
constexpr int G1_NST = 3;

__global__ __launch_bounds__(256) void k_gemm1(const u16* __restrict__ x, const u16* __restrict__ Wg, const u16* __restrict__ Wu,
                                               char* __restrict__ Hf) {
    extern __shared__ __attribute__((aligned(16))) char lds[];
    const int t = threadIdx.x, lane = t & 63, w = __builtin_amdgcn_readfirstlane(t >> 6);
    const int b = blockIdx.x;
    const int xcd = b & 7, c = b >> 3;
    const int pi = (xcd & 1) * 8 + (c & 7), tj = (xcd >> 1) * 4 + (c >> 3);
    const int sg = tj <= 10 ? 5 * tj : 50 + 6 * (tj - 10), ng = tj < 10 ? 5 : 6;
    const rsrc_t rx = mkrsrc(x, L_ * D_ * 2), rg = mkrsrc(Wg, D_ * F_ * 2), ru = mkrsrc(Wu, D_ * F_ * 2);
    const unsigned vx = (8 * w + (lane >> 3)) * 1024 + (((lane & 7) ^ ((4 * w + (lane >> 4)) & 7)) << 4);
    unsigned vw[3];
#pragma unroll
    for (int i = 0; i < 3; ++i) {
        int cc = 64 * i + lane, row = cc / 12, ch = cc % 12;
        if (ng == 5 && ch >= 10) ch -= 2;
        vw[i] = row * (F_ * 2) + ch * 16;
    }
    const unsigned sx0 = pi * 128 * 1024, sw0 = w * 16 * (F_ * 2) + sg * 32;
    auto stage = [&](int kt, int buf) {
        char* base = lds + buf * G1_STAGE;
#pragma unroll
        for (int i = 0; i < 4; ++i) glds16(rx, base + G1_X + (w + 4 * i) * 1024, vx, sx0 + kt * 128 + i * 32768);
#pragma unroll
        for (int i = 0; i < 3; ++i) glds16(rg, base + G1_WG + (3 * w + i) * 1024, vw[i], sw0 + kt * 64 * (F_ * 2));
#pragma unroll
        for (int i = 0; i < 3; ++i) glds16(ru, base + G1_WU + (3 * w + i) * 1024, vw[i], sw0 + kt * 64 * (F_ * 2));
    };
    const int wf = w & 1, wm = w >> 1;
    const int m = lane & 31, hh = lane >> 5, grp = lane >> 4, q = (lane >> 2) & 3, p = lane & 3;
    const int xoff = G1_X + (64 * wm + m) * 128 + ((hh ^ ((m >> 1) & 7)) << 4);
    const int woff = ((grp & 1) ? G1_WU : G1_WG) + (8 * hh + q) * 192 + p * 8 + wf * 96;
    v16f acc[3][2];
#pragma unroll
    for (int j = 0; j < 3; ++j) { acc[j][0] = (v16f){}; acc[j][1] = (v16f){}; }
    stage(0, 0); stage(1, 1);
    int buf = 0;
#pragma unroll 1
    for (int kt = 0; kt < 8; ++kt) {
        if (kt < 7) { WAIT_VM(10); } else { WAIT_VM(0); }
        BAR();
        if (kt + 2 < 8) { int nb = buf + 2; if (nb >= 3) nb -= 3; stage(kt + 2, nb); }
        const char* base = lds + buf * G1_STAGE;
#pragma unroll
        for (int s = 0; s < 4; ++s) {
            v8bf fb[2], fa[3];
#pragma unroll
            for (int mt = 0; mt < 2; ++mt) fb[mt] = *(const v8bf*)(base + ((xoff ^ (s << 5)) + mt * 4096));
#pragma unroll
            for (int j = 0; j < 3; ++j) fa[j] = tr_frag(base + woff + (16 * s) * 192 + j * 32, base + woff + (16 * s + 4) * 192 + j * 32);
#pragma unroll
            for (int j = 0; j < 3; ++j)
#pragma unroll
                for (int mt = 0; mt < 2; ++mt)
                    acc[j][mt] = __builtin_amdgcn_mfma_f32_32x32x16_bf16(fa[j], fb[mt], acc[j][mt], 0, 0, 0);
        }
        WAIT_LGKM0();
        buf = buf == 2 ? 0 : buf + 1;
    }
    const float c1 = -2.f * 0.7978845608028654f * 1.44269504088896341f, c2 = c1 * 0.044715f;
#pragma unroll
    for (int j = 0; j < 3; ++j) {
        const int gj = 3 * wf + j;
        if (gj < ng) {
#pragma unroll
            for (int mt = 0; mt < 2; ++mt) {
                float h[8];
#pragma unroll
                for (int e = 0; e < 8; ++e) {
                    float g = acc[j][mt][e], uu = acc[j][mt][8 + e];
                    float ex = __builtin_amdgcn_exp2f(g * (c1 + c2 * g * g));
                    h[e] = g * uu * __builtin_amdgcn_rcpf(1.f + ex);
                }
                v4u o = {pk2(h[0], h[1]), pk2(h[2], h[3]), pk2(h[4], h[5]), pk2(h[6], h[7])};
                *(v4u*)(Hf + ((size_t)(pi * 4 + 2 * wm + mt) * NG + sg + gj) * 1024 + lane * 16) = o;
            }
        }
    }
}

constexpr int G2_DEPTH = 8;
__global__ __launch_bounds__(256) void k_gemm2(const char* __restrict__ Hf, const u16* __restrict__ Wd, const unsigned* __restrict__ a2,
                                               const float* __restrict__ gin, u16* __restrict__ out) {
    extern __shared__ __attribute__((aligned(16))) char lds[];
    const int t = threadIdx.x, lane = t & 63, w = __builtin_amdgcn_readfirstlane(t >> 6);
    const int b = blockIdx.x;
    const int xcd = b & 7, c = b >> 3;
    const int im = xcd * 4 + (c >> 3), jd = c & 7;
    const int g0 = w < 2 ? 22 * w : 44 + 21 * (w - 2), ngr = w < 2 ? 22 : 21;
    const rsrc_t rh = mkrsrc(Hf, 64 * NG * 1024), rw = mkrsrc(Wd, F_ * D_ * 2);
    const unsigned vh = lane * 16;
    const unsigned vw = (lane >> 3) * 1024 + (((lane & 7) ^ (((lane >> 4) & 1) << 2)) << 4);
    char* ring = lds + w * (G2_DEPTH * 4096);
    auto stage = [&](int g, int slot) {
        char* base = ring + slot * 4096;
        glds16(rh, base, vh, ((2 * im) * NG + g) * 1024);
        glds16(rh, base + 1024, vh, ((2 * im + 1) * NG + g) * 1024);
        glds16(rw, base + 2048, vw, g * 16 * 1024 + jd * 128);
        glds16(rw, base + 3072, vw, g * 16 * 1024 + 8192 + jd * 128);
    };
    const int hh = lane >> 5, grp = lane >> 4, q = (lane >> 2) & 3, p = lane & 3;
    const int woff = 2048 + (4 * hh + q) * 128 + ((q >> 1) << 6) + (grp & 1) * 32 + p * 8;
    v16f acc[2][2];
#pragma unroll
    for (int j = 0; j < 2; ++j) { acc[j][0] = (v16f){}; acc[j][1] = (v16f){}; }
#pragma unroll
    for (int i = 0; i < G2_DEPTH - 1; ++i) stage(g0 + i, i);
#pragma unroll 1
    for (int i = 0; i < ngr; ++i) {
        int gn = i + G2_DEPTH - 1; if (gn >= ngr) gn = ngr - 1;
        WAIT_LGKM0();
        stage(g0 + gn, (i + G2_DEPTH - 1) & (G2_DEPTH - 1));
        WAIT_VM(28);
        const char* base = ring + (i & (G2_DEPTH - 1)) * 4096;
        v8bf fb[2], fa[2];
#pragma unroll
        for (int mt = 0; mt < 2; ++mt) fb[mt] = *(const v8bf*)(base + mt * 1024 + lane * 16);
#pragma unroll
        for (int dt = 0; dt < 2; ++dt) fa[dt] = tr_frag(base + (woff ^ (dt << 6)), base + (woff ^ (dt << 6)) + 1024);
#pragma unroll
        for (int dt = 0; dt < 2; ++dt)
#pragma unroll
            for (int mt = 0; mt < 2; ++mt)
                acc[dt][mt] = __builtin_amdgcn_mfma_f32_32x32x16_bf16(fa[dt], fb[mt], acc[dt][mt], 0, 0, 0);
    }
    WAIT_VM(0);
    __syncthreads();
#pragma unroll
    for (int dt = 0; dt < 2; ++dt)
#pragma unroll
        for (int mt = 0; mt < 2; ++mt)
#pragma unroll
            for (int k = 0; k < 4; ++k) {
                v4f v = {acc[dt][mt][4 * k], acc[dt][mt][4 * k + 1], acc[dt][mt][4 * k + 2], acc[dt][mt][4 * k + 3]};
                *(v4f*)(lds + ((dt + 2 * mt) * 4 + w) * 4096 + k * 1024 + lane * 16) = v;
            }
    __syncthreads();
    const int dt = w & 1, mt = w >> 1;
    const int l = im * 64 + mt * 32 + (lane & 31);
    const float g = gin[l], og = rbf(1.f - g);
#pragma unroll
    for (int k = 0; k < 4; ++k) {
        v4f s = {0, 0, 0, 0};
#pragma unroll
        for (int src = 0; src < 4; ++src) s += *(const v4f*)(lds + (w * 4 + src) * 4096 + k * 1024 + lane * 16);
        const int d0 = jd * 64 + dt * 32 + 8 * k + 4 * hh;
        const unsigned a01 = a2[(size_t)(d0 >> 1) * L_ + l], a23 = a2[(size_t)((d0 >> 1) + 1) * L_ + l];
        unsigned b01 = pk2(s[0], s[1]), b23 = pk2(s[2], s[3]);
        unsigned ga01 = pk2(g * bflo(a01), g * bfhi(a01)), ga23 = pk2(g * bflo(a23), g * bfhi(a23));
        unsigned gb01 = pk2(og * bflo(b01), og * bfhi(b01)), gb23 = pk2(og * bflo(b23), og * bfhi(b23));
        v2u o = {pk2(bflo(ga01) + bflo(gb01), bfhi(ga01) + bfhi(gb01)), pk2(bflo(ga23) + bflo(gb23), bfhi(ga23) + bfhi(gb23))};
        *(v2u*)(out + (size_t)l * D_ + d0) = o;
    }
}

extern "C" void kernel_launch(void* const* d_in, const int* in_sizes, int n_in,
                              void* d_out, int out_size, void* d_ws, size_t ws_size,
                              hipStream_t stream) {
    const u16* u = (const u16*)d_in[0];
    char* ws = (char*)d_ws;
    (void)hipFuncSetAttribute((const void*)k_ssm, hipFuncAttributeMaxDynamicSharedMemorySize, S_END);
    (void)hipFuncSetAttribute((const void*)k_gemm1, hipFuncAttributeMaxDynamicSharedMemorySize, G1_NST * G1_STAGE);
    (void)hipFuncSetAttribute((const void*)k_gemm2, hipFuncAttributeMaxDynamicSharedMemorySize, 4 * G2_DEPTH * 4096);
    k_ssm<<<256, 256, S_END, stream>>>(u, (const u16*)d_in[1], (const u16*)d_in[2], (const u16*)d_in[3], (const u16*)d_in[4],
        (const u16*)d_in[5], (const u16*)d_in[6], (const u16*)d_in[7], (const u16*)d_in[8], (const u16*)d_in[13],
        (unsigned*)(ws + WS_A), (float*)(ws + WS_G));
    k_gemm1<<<256, 256, G1_NST * G1_STAGE, stream>>>(u, (const u16*)d_in[10], (const u16*)d_in[11], ws + WS_H);
    k_gemm2<<<256, 256, 4 * G2_DEPTH * 4096, stream>>>(ws + WS_H, (const u16*)d_in[12], (const unsigned*)(ws + WS_A),
        (const float*)(ws + WS_G), (u16*)d_out);
}
```

```cpp
#define STAMPS
#define PK0 0
#define PK1 0
#define PM0 0
#define PM1 0
#define PMULT 0
#define PB0 0
#define PB1 STAMP_BLOCKS
#include <hip/hip_runtime.h>
#include <hip/hip_bf16.h>
#include <stdint.h>

#define LAS __attribute__((address_space(3)))
#define GAS __attribute__((address_space(1)))
typedef short  v4s  __attribute__((ext_vector_type(4)));
typedef short  v8s  __attribute__((ext_vector_type(8)));
typedef __bf16 v8bf __attribute__((ext_vector_type(8)));
typedef __bf16 v2bf __attribute__((ext_vector_type(2)));
typedef float  v2f  __attribute__((ext_vector_type(2)));
typedef float  v4f  __attribute__((ext_vector_type(4)));
typedef float  v16f __attribute__((ext_vector_type(16)));
typedef unsigned v4u __attribute__((ext_vector_type(4)));
typedef unsigned v2u __attribute__((ext_vector_type(2)));
typedef unsigned short u16;

constexpr int L_ = 2048, D_ = 512, N_ = 128, F_ = 1376;
constexpr int NG = F_ / 16;

constexpr size_t WS_H = 0;
constexpr size_t WS_A = WS_H + (size_t)64 * NG * 1024;
constexpr size_t WS_G = WS_A + (size_t)256 * 2048 * 4;

__device__ __forceinline__ float bf2f(u16 v) { return __uint_as_float((unsigned)v << 16); }
__device__ __forceinline__ float bflo(unsigned v) { return __uint_as_float(v << 16); }
__device__ __forceinline__ float bfhi(unsigned v) { return __uint_as_float(v & 0xffff0000u); }
__device__ __forceinline__ unsigned pk2(float lo, float hi) {
    v2f f = {lo, hi};
    return __builtin_bit_cast(unsigned, __builtin_convertvector(f, v2bf));
}
__device__ __forceinline__ float rbf(float x) { return bflo(pk2(x, x)); }

#define WAIT_VM(n)  asm volatile("s_waitcnt vmcnt(" #n ")" ::: "memory")
#define WAIT_LGKM0() asm volatile("s_waitcnt lgkmcnt(0)" ::: "memory")
#define BAR() __builtin_amdgcn_s_barrier()

typedef v4u rsrc_t;
__device__ __forceinline__ rsrc_t mkrsrc(const void* p, unsigned bytes) {
    uint64_t a = (uint64_t)p;
    rsrc_t r = {(unsigned)a, (unsigned)(a >> 32) & 0xffffu, bytes, 0x00020000u};
    return r;
}
__device__ __forceinline__ unsigned lds_addr(const char* p) { return (unsigned)(uintptr_t)(LAS const char*)p; }
__device__ __forceinline__ void glds16(rsrc_t r, char* lds_dst, unsigned voff, unsigned soff) {
    asm volatile("s_mov_b32 m0, %0\n\ts_nop 0\n\tbuffer_load_dwordx4 %1, %2, %3 offen lds"
                 :: "s"(lds_addr(lds_dst)), "v"(voff), "s"(r), "s"(soff) : "memory");
}
__device__ __forceinline__ v8bf tr_frag(const char* p0, const char* p1) {
    v4s a = __builtin_amdgcn_ds_read_tr16_b64_v4i16((LAS v4s*)p0);
    v4s b = __builtin_amdgcn_ds_read_tr16_b64_v4i16((LAS v4s*)p1);
    v8s r = __builtin_shufflevector(a, b, 0, 1, 2, 3, 4, 5, 6, 7);
    return __builtin_bit_cast(v8bf, r);
}

constexpr size_t WS_STAMP = (size_t)200 << 20;
constexpr int NSTAMP = 32, STAMP_BLOCKS = 256;
#define STAMP(base, k) do { if (threadIdx.x == 0) { unsigned long long t_; asm volatile("s_memrealtime %0\n\ts_waitcnt lgkmcnt(0)" : "=s"(t_) :: "memory"); ((unsigned long long*)(base))[blockIdx.x * NSTAMP + (k)] = t_; } } while (0)
#define STAMPC(base, k) do { if (threadIdx.x == 0) { unsigned long long t_; asm volatile("s_memtime %0\n\ts_waitcnt lgkmcnt(0)" : "=s"(t_) :: "memory"); ((unsigned long long*)(base))[blockIdx.x * NSTAMP + (k)] = t_; } } while (0)
struct cf { float r, i; };
__device__ __forceinline__ cf cmul(cf a, cf b) { return {a.r * b.r - a.i * b.i, a.r * b.i + a.i * b.r}; }
__device__ __forceinline__ cf cexp_(float al, float be, float e) {
    float m = __builtin_amdgcn_exp2f(al * e);
    float th = __builtin_amdgcn_fractf(be * e);
    return {m * __builtin_amdgcn_cosf(th), m * __builtin_amdgcn_sinf(th)};
}

constexpr int TP = 272;
constexpr int S_T = 0;
constexpr int S_TW = 53 * TP;
constexpr int S_KC = 0;
constexpr int KCP = 4160;
constexpr int S_Y = 0;
constexpr int S_KD = 4 * S_TW;
constexpr int KDN = 704;
constexpr int S_U = S_KD + 4 * KDN * 4;
constexpr int UP = 2288 * 2;
constexpr int S_END = S_U + 2 * UP;

__global__ __launch_bounds__(256) void k_ssm(const u16* __restrict__ u, const u16* __restrict__ Are, const u16* __restrict__ Aim,
        const u16* __restrict__ Bre, const u16* __restrict__ Bim, const u16* __restrict__ Cre, const u16* __restrict__ Cim,
        const u16* __restrict__ logdt, const u16* __restrict__ Dskip, const u16* __restrict__ gw,
        unsigned* __restrict__ a2, float* __restrict__ gout) {
    extern __shared__ __attribute__((aligned(16))) char lds[];
    const int t = threadIdx.x, lane = t & 63, w = __builtin_amdgcn_readfirstlane(t >> 6);
    const int b = blockIdx.x, ch = w >> 1, hf = w & 1;
    const int d = 2 * b + ch;
    STAMP(((char*)a2 - WS_A + WS_STAMP), 0);
    unsigned uu[8];
#pragma unroll
    for (int i = 0; i < 8; ++i) uu[i] = *(const unsigned*)(u + (size_t)(t * 8 + i) * D_ + 2 * b);
    {
        const int row = 8 * b + 2 * w + (lane >> 5), i = lane & 31;
        const v4u x0 = *(const v4u*)(u + (size_t)row * D_ + 16 * i), x1 = *(const v4u*)(u + (size_t)row * D_ + 16 * i + 8);
        const v4u g0 = *(const v4u*)(gw + 16 * i), g1 = *(const v4u*)(gw + 16 * i + 8);
        float s = 0.f;
#pragma unroll
        for (int k = 0; k < 4; ++k) {
            unsigned p0 = pk2(bflo(x0[k]) * bflo(g0[k]), bfhi(x0[k]) * bfhi(g0[k]));
            unsigned p1 = pk2(bflo(x1[k]) * bflo(g1[k]), bfhi(x1[k]) * bfhi(g1[k]));
            s += bflo(p0) + bfhi(p0) + bflo(p1) + bfhi(p1);
        }
#pragma unroll
        for (int m = 16; m >= 1; m >>= 1) s += __shfl_xor(s, m);
        s = rbf(s);
        float g = rbf(1.f / (1.f + __expf(-s)));
        if (i == 0) gout[row] = g;
    }
    STAMP(((char*)a2 - WS_A + WS_STAMP), 1);
    const int n = 64 * hf + lane, pi = d * N_ + n;
    const float dt = rbf(__expf(bf2f(logdt[d])));
    const float al = dt * bf2f(Are[pi]) * 1.44269504088896341f, be = dt * bf2f(Aim[pi]) * 0.15915494309189535f;
    const cf wv = cmul(cf{bf2f(Cre[pi]), bf2f(Cim[pi])}, cf{dt * bf2f(Bre[pi]), dt * bf2f(Bim[pi])});
    char* T = lds + S_T + w * S_TW + lane * 4;
    {
        const cf z1 = cexp_(al, be, 1.f), z8 = cexp_(al, be, 8.f), z256 = cexp_(al, be, 256.f);
        const cf z16 = cmul(z8, z8);
        cf e = {1.f, 0.f}, o = z8;
#pragma unroll
        for (int r = 0; r < 32; r += 2) {
            *(unsigned*)(T + r * TP) = pk2(e.r, e.i);
            *(unsigned*)(T + (r + 1) * TP) = pk2(o.r, o.i);
            if (r + 2 < 32) { e = cmul(e, z16); o = cmul(o, z16); }
        }
        char* T2 = T + 32 * TP;
        cf p = wv;
#pragma unroll
        for (int c = 0; c < 8; ++c) { *(unsigned*)(T2 + c * TP) = pk2(p.r, -p.i); if (c < 7) p = cmul(p, z1); }
        const cf z2 = cmul(z1, z1), z4 = cmul(z2, z2);
        cf q = cmul(wv, z256);
        cf q2 = q;
#pragma unroll
        for (int c = 0; c < 4; ++c) { *(unsigned*)(T2 + (8 + c) * TP) = pk2(q2.r, -q2.i); if (c < 3) q2 = cmul(q2, z2); }
#pragma unroll
        for (int c = 0; c < 2; ++c) {
            q = cmul(q, z256);
            cf q4 = cmul(q, z4);
            *(unsigned*)(T2 + (12 + 2 * c) * TP) = pk2(q.r, -q.i);
            *(unsigned*)(T2 + (13 + 2 * c) * TP) = pk2(q4.r, -q4.i);
        }
#pragma unroll
        for (int c = 0; c < 5; ++c) { q = cmul(q, z256); *(unsigned*)(T2 + (16 + c) * TP) = pk2(q.r, -q.i); }
    }
    STAMP(((char*)a2 - WS_A + WS_STAMP), 2);
    {
        v4u p0, p1;
#pragma unroll
        for (int i = 0; i < 4; ++i) {
            p0[i] = (uu[2 * i] & 0xffffu) | (uu[2 * i + 1] << 16);
            p1[i] = (uu[2 * i] >> 16) | (uu[2 * i + 1] & 0xffff0000u);
        }
        *(v4u*)(lds + S_U + (240 + 8 * t) * 2) = p0;
        *(v4u*)(lds + S_U + UP + (240 + 8 * t) * 2) = p1;
        if (t < 60) *(v4u*)(lds + S_U + (t >= 30 ? UP : 0) + (t % 30) * 16) = (v4u){0, 0, 0, 0};
    }
    STAMP(((char*)a2 - WS_A + WS_STAMP), 3);
    v16f kacc = {};
    {
        const int r = lane & 31, hh = lane >> 5;
        const char* TA = lds + S_T + w * S_TW + r * TP + hh * 16;
        const char* TB = lds + S_T + w * S_TW + (32 + (r < 20 ? r : 20)) * TP + hh * 16;
#pragma unroll
        for (int s = 0; s < 8; ++s) {
            v8bf fa = *(const v8bf*)(TA + s * 32), fb = *(const v8bf*)(TB + s * 32);
            kacc = __builtin_amdgcn_mfma_f32_32x32x16_bf16(fa, fb, kacc, 0, 0, 0);
        }
        const int c = r;
        int base, stride;
        if (c < 8) { base = c; stride = 8; }
        else if (c < 12) { base = 256 + (c - 8); stride = 4; }
        else if (c < 16) { base = 384 + 64 * ((c - 12) >> 1) + ((c - 12) & 1); stride = 2; }
        else if (c <= 20) { base = 512 + 32 * (c - 16); stride = 1; }
        else { base = 672; stride = 1; }
        float* KD = (float*)(lds + S_KD) + w * KDN;
#pragma unroll
        for (int q = 0; q < 16; ++q) KD[base + stride * ((q & 3) + 8 * (q >> 2) + 4 * hh)] = kacc[q];
    }
    __syncthreads();
    STAMP(((char*)a2 - WS_A + WS_STAMP), 4);
#pragma unroll
    for (int c = 0; c < 2; ++c) {
        const float* K0 = (const float*)(lds + S_KD) + (2 * c) * KDN;
        const float* K1 = K0 + KDN;
        float v[12];
#pragma unroll
        for (int k = 0; k < 12; ++k) {
            int j = 2047 - (8 * t + k);
            int jj = j < 0 ? 0 : j;
            int idx = jj < 128 ? jj : (int)(pk2((float)jj, 0.f) & 0xffffu) - 17024;
            float x = K0[idx] + K1[idx];
            v[k] = j < 0 ? 0.f : x;
        }
#pragma unroll
        for (int s = 0; s < 4; ++s) {
            v4u o = {pk2(v[s], v[s + 1]), pk2(v[s + 2], v[s + 3]), pk2(v[s + 4], v[s + 5]), pk2(v[s + 6], v[s + 7])};
            *(v4u*)(lds + S_KC + (c * 4 + s) * KCP + t * 16) = o;
        }
        if (t < 8) *(v4u*)(lds + S_KC + (c * 4 + (t >> 1)) * KCP + 4096 + (t & 1) * 16) = (v4u){0, 0, 0, 0};
    }
    __syncthreads();
    STAMP(((char*)a2 - WS_A + WS_STAMP), 5);
    v4f y[8];
#pragma unroll
    for (int p = 0; p < 8; ++p) y[p] = (v4f){0, 0, 0, 0};
    {
        const int r = lane & 15, g = lane >> 4, rp = 15 - r;
        const char* KA = lds + S_KC + (ch * 4 + (rp & 3)) * KCP + (8 * g + (rp & 12) + 128 * hf) * 2;
        const char* UB = lds + S_U + ch * UP + (16 * r + 8 * g + 128 * hf) * 2;
#pragma unroll 1
        for (int tt = 0; tt < 4; ++tt) {
            v8bf fa[8], fb[8];
#pragma unroll
            for (int m = 0; m < 8; ++m) {
                v2u lo = *(const v2u*)(KA + tt * 64 + (7 - m) * 512), hi = *(const v2u*)(KA + tt * 64 + (7 - m) * 512 + 8);
                v4u x = {lo[0], lo[1], hi[0], hi[1]};
                fa[m] = __builtin_bit_cast(v8bf, x);
                fb[m] = *(const v8bf*)(UB + tt * 64 + m * 512);
            }
#pragma unroll
            for (int m = 0; m < 8; ++m)
#pragma unroll
                for (int q = 0; q + m < 8; ++q)
                    y[m + q] = __builtin_amdgcn_mfma_f32_16x16x32_bf16(fa[m], fb[q], y[m + q], 0, 0, 0);
        }
    }
    STAMP(((char*)a2 - WS_A + WS_STAMP), 6);
    __syncthreads();
#pragma unroll
    for (int p = 0; p < 8; ++p) *(v4f*)(lds + S_Y + w * 8192 + (p * 64 + lane) * 16) = y[p];
    __syncthreads();
    {
        const int p = t >> 5, l1 = (t >> 1) & 15, g0 = 2 * (t & 1);
        const float ds0 = bf2f(Dskip[2 * b]), ds1 = bf2f(Dskip[2 * b + 1]);
        v4u o[2];
#pragma unroll
        for (int h = 0; h < 2; ++h) {
            const char* yp = lds + S_Y + (p * 64 + (g0 + h) * 16 + l1) * 16;
            v4f y0 = *(const v4f*)(yp) + *(const v4f*)(yp + 8192);
            v4f y1 = *(const v4f*)(yp + 16384) + *(const v4f*)(yp + 24576);
#pragma unroll
            for (int k = 0; k < 4; ++k) {
                unsigned yb = pk2(y0[k], y1[k]);
                unsigned sk = pk2(bflo(uu[4 * h + k]) * ds0, bfhi(uu[4 * h + k]) * ds1);
                o[h][k] = pk2(bflo(yb) + bflo(sk), bfhi(yb) + bfhi(sk));
            }
        }
        v4u* dst = (v4u*)(a2 + (size_t)b * L_ + 8 * t);
        dst[0] = o[0]; dst[1] = o[1];
    }
    STAMP(((char*)a2 - WS_A + WS_STAMP), 7);
}

constexpr int G1_X = 0, G1_WG = 16384, G1_WU = G1_WG + 12288 + 32, G1_STAGE = 40960 + 64;
constexpr int G1_NST = 3;

__global__ __launch_bounds__(256) void k_gemm1(const u16* __restrict__ x, const u16* __restrict__ Wg, const u16* __restrict__ Wu,
                                               char* __restrict__ Hf) {
    extern __shared__ __attribute__((aligned(16))) char lds[];
    const int t = threadIdx.x, lane = t & 63, w = __builtin_amdgcn_readfirstlane(t >> 6);
    const int b = blockIdx.x;
    const int xcd = b & 7, c = b >> 3;
    STAMP(((char*)Hf - WS_H + WS_STAMP), 8);
    const int pi = (xcd & 1) * 8 + (c & 7), tj = (xcd >> 1) * 4 + (c >> 3);
    const int sg = tj <= 10 ? 5 * tj : 50 + 6 * (tj - 10), ng = tj < 10 ? 5 : 6;
    const rsrc_t rx = mkrsrc(x, L_ * D_ * 2), rg = mkrsrc(Wg, D_ * F_ * 2), ru = mkrsrc(Wu, D_ * F_ * 2);
    const unsigned vx = (8 * w + (lane >> 3)) * 1024 + (((lane & 7) ^ ((4 * w + (lane >> 4)) & 7)) << 4);
    unsigned vw[3];
#pragma unroll
    for (int i = 0; i < 3; ++i) {
        int cc = 64 * i + lane, row = cc / 12, ch = cc % 12;
        if (ng == 5 && ch >= 10) ch -= 2;
        vw[i] = row * (F_ * 2) + ch * 16;
    }
    const unsigned sx0 = pi * 128 * 1024, sw0 = w * 16 * (F_ * 2) + sg * 32;
    auto stage = [&](int kt, int buf) {
        char* base = lds + buf * G1_STAGE;
#pragma unroll
        for (int i = 0; i < 4; ++i) glds16(rx, base + G1_X + (w + 4 * i) * 1024, vx, sx0 + kt * 128 + i * 32768);
#pragma unroll
        for (int i = 0; i < 3; ++i) glds16(rg, base + G1_WG + (3 * w + i) * 1024, vw[i], sw0 + kt * 64 * (F_ * 2));
#pragma unroll
        for (int i = 0; i < 3; ++i) glds16(ru, base + G1_WU + (3 * w + i) * 1024, vw[i], sw0 + kt * 64 * (F_ * 2));
    };
    const int wf = w & 1, wm = w >> 1;
    const int m = lane & 31, hh = lane >> 5, grp = lane >> 4, q = (lane >> 2) & 3, p = lane & 3;
    const int xoff = G1_X + (64 * wm + m) * 128 + ((hh ^ ((m >> 1) & 7)) << 4);
    const int woff = ((grp & 1) ? G1_WU : G1_WG) + (8 * hh + q) * 192 + p * 8 + wf * 96;
    v16f acc[3][2];
#pragma unroll
    for (int j = 0; j < 3; ++j) { acc[j][0] = (v16f){}; acc[j][1] = (v16f){}; }
    stage(0, 0); stage(1, 1);
    int buf = 0;
#pragma unroll 1
    for (int kt = 0; kt < 8; ++kt) {
        if (kt < 7) { WAIT_VM(10); } else { WAIT_VM(0); }
        BAR();
        if (kt + 2 < 8) { int nb = buf + 2; if (nb >= 3) nb -= 3; stage(kt + 2, nb); }
        const char* base = lds + buf * G1_STAGE;
#pragma unroll
        for (int s = 0; s < 4; ++s) {
            v8bf fb[2], fa[3];
#pragma unroll
            for (int mt = 0; mt < 2; ++mt) fb[mt] = *(const v8bf*)(base + ((xoff ^ (s << 5)) + mt * 4096));
#pragma unroll
            for (int j = 0; j < 3; ++j) fa[j] = tr_frag(base + woff + (16 * s) * 192 + j * 32, base + woff + (16 * s + 4) * 192 + j * 32);
#pragma unroll
            for (int j = 0; j < 3; ++j)
#pragma unroll
                for (int mt = 0; mt < 2; ++mt)
                    acc[j][mt] = __builtin_amdgcn_mfma_f32_32x32x16_bf16(fa[j], fb[mt], acc[j][mt], 0, 0, 0);
        }
        WAIT_LGKM0();
        buf = buf == 2 ? 0 : buf + 1;
    }
    STAMP(((char*)Hf - WS_H + WS_STAMP), 17);
    const float c1 = -2.f * 0.7978845608028654f * 1.44269504088896341f, c2 = c1 * 0.044715f;
#pragma unroll
    for (int j = 0; j < 3; ++j) {
        const int gj = 3 * wf + j;
        if (gj < ng) {
#pragma unroll
            for (int mt = 0; mt < 2; ++mt) {
                float h[8];
#pragma unroll
                for (int e = 0; e < 8; ++e) {
                    float g = acc[j][mt][e], uu = acc[j][mt][8 + e];
                    float ex = __builtin_amdgcn_exp2f(g * (c1 + c2 * g * g));
                    h[e] = g * uu * __builtin_amdgcn_rcpf(1.f + ex);
                }
                v4u o = {pk2(h[0], h[1]), pk2(h[2], h[3]), pk2(h[4], h[5]), pk2(h[6], h[7])};
                *(v4u*)(Hf + ((size_t)(pi * 4 + 2 * wm + mt) * NG + sg + gj) * 1024 + lane * 16) = o;
            }
        }
    }
    STAMP(((char*)Hf - WS_H + WS_STAMP), 18);
}

constexpr int G2_DEPTH = 8;
__global__ __launch_bounds__(256) void k_gemm2(const char* __restrict__ Hf, const u16* __restrict__ Wd, const unsigned* __restrict__ a2,
                                               const float* __restrict__ gin, u16* __restrict__ out) {
    extern __shared__ __attribute__((aligned(16))) char lds[];
    const int t = threadIdx.x, lane = t & 63, w = __builtin_amdgcn_readfirstlane(t >> 6);
    const int b = blockIdx.x;
    const int xcd = b & 7, c = b >> 3;
    STAMP(((char*)Hf - WS_H + WS_STAMP), 20);
    const int im = xcd * 4 + (c >> 3), jd = c & 7;
    const int g0 = w < 2 ? 22 * w : 44 + 21 * (w - 2), ngr = w < 2 ? 22 : 21;
    const rsrc_t rh = mkrsrc(Hf, 64 * NG * 1024), rw = mkrsrc(Wd, F_ * D_ * 2);
    const unsigned vh = lane * 16;
    const unsigned vw = (lane >> 3) * 1024 + (((lane & 7) ^ (((lane >> 4) & 1) << 2)) << 4);
    char* ring = lds + w * (G2_DEPTH * 4096);
    auto stage = [&](int g, int slot) {
        char* base = ring + slot * 4096;
        glds16(rh, base, vh, ((2 * im) * NG + g) * 1024);
        glds16(rh, base + 1024, vh, ((2 * im + 1) * NG + g) * 1024);
        glds16(rw, base + 2048, vw, g * 16 * 1024 + jd * 128);
        glds16(rw, base + 3072, vw, g * 16 * 1024 + 8192 + jd * 128);
    };
    const int hh = lane >> 5, grp = lane >> 4, q = (lane >> 2) & 3, p = lane & 3;
    const int woff = 2048 + (4 * hh + q) * 128 + ((q >> 1) << 6) + (grp & 1) * 32 + p * 8;
    v16f acc[2][2];
#pragma unroll
    for (int j = 0; j < 2; ++j) { acc[j][0] = (v16f){}; acc[j][1] = (v16f){}; }
#pragma unroll
    for (int i = 0; i < G2_DEPTH - 1; ++i) stage(g0 + i, i);
#pragma unroll 1
    for (int i = 0; i < ngr; ++i) {
        int gn = i + G2_DEPTH - 1; if (gn >= ngr) gn = ngr - 1;
        WAIT_LGKM0();
        stage(g0 + gn, (i + G2_DEPTH - 1) & (G2_DEPTH - 1));
        WAIT_VM(28);
        const char* base = ring + (i & (G2_DEPTH - 1)) * 4096;
        v8bf fb[2], fa[2];
#pragma unroll
        for (int mt = 0; mt < 2; ++mt) fb[mt] = *(const v8bf*)(base + mt * 1024 + lane * 16);
#pragma unroll
        for (int dt = 0; dt < 2; ++dt) fa[dt] = tr_frag(base + (woff ^ (dt << 6)), base + (woff ^ (dt << 6)) + 1024);
#pragma unroll
        for (int dt = 0; dt < 2; ++dt)
#pragma unroll
            for (int mt = 0; mt < 2; ++mt)
                acc[dt][mt] = __builtin_amdgcn_mfma_f32_32x32x16_bf16(fa[dt], fb[mt], acc[dt][mt], 0, 0, 0);
    }
    STAMP(((char*)Hf - WS_H + WS_STAMP), 23);
    WAIT_VM(0);
    __syncthreads();
    STAMP(((char*)Hf - WS_H + WS_STAMP), 24);
#pragma unroll
    for (int dt = 0; dt < 2; ++dt)
#pragma unroll
        for (int mt = 0; mt < 2; ++mt)
#pragma unroll
            for (int k = 0; k < 4; ++k) {
                v4f v = {acc[dt][mt][4 * k], acc[dt][mt][4 * k + 1], acc[dt][mt][4 * k + 2], acc[dt][mt][4 * k + 3]};
                *(v4f*)(lds + ((dt + 2 * mt) * 4 + w) * 4096 + k * 1024 + lane * 16) = v;
            }
    __syncthreads();
    const int dt = w & 1, mt = w >> 1;
    const int l = im * 64 + mt * 32 + (lane & 31);
    const float g = gin[l], og = rbf(1.f - g);
#pragma unroll
    for (int k = 0; k < 4; ++k) {
        v4f s = {0, 0, 0, 0};
#pragma unroll
        for (int src = 0; src < 4; ++src) s += *(const v4f*)(lds + (w * 4 + src) * 4096 + k * 1024 + lane * 16);
        const int d0 = jd * 64 + dt * 32 + 8 * k + 4 * hh;
        const unsigned a01 = a2[(size_t)(d0 >> 1) * L_ + l], a23 = a2[(size_t)((d0 >> 1) + 1) * L_ + l];
        unsigned b01 = pk2(s[0], s[1]), b23 = pk2(s[2], s[3]);
        unsigned ga01 = pk2(g * bflo(a01), g * bfhi(a01)), ga23 = pk2(g * bflo(a23), g * bfhi(a23));
        unsigned gb01 = pk2(og * bflo(b01), og * bfhi(b01)), gb23 = pk2(og * bflo(b23), og * bfhi(b23));
        v2u o = {pk2(bflo(ga01) + bflo(gb01), bfhi(ga01) + bfhi(gb01)), pk2(bflo(ga23) + bflo(gb23), bfhi(ga23) + bfhi(gb23))};
        *(v2u*)(out + (size_t)l * D_ + d0) = o;
    }
    STAMP(((char*)Hf - WS_H + WS_STAMP), 25);
}

__global__ void k_probe(const unsigned long long* st) {
    __shared__ unsigned long long a0[STAMP_BLOCKS], a1[STAMP_BLOCKS];
    for (int i = threadIdx.x; i < STAMP_BLOCKS; i += blockDim.x) { a0[i] = st[i * NSTAMP + PK0]; a1[i] = st[i * NSTAMP + PK1]; }
    __syncthreads();
    if (threadIdx.x == 0) {
        unsigned long long ref = a0[PB0];
        long long mn0 = 1ll << 60, mx0 = -(1ll << 60), s0 = 0, mn1 = 1ll << 60, mx1 = -(1ll << 60), s1 = 0;
        for (int i = PB0; i < PB1; ++i) { long long x = (long long)(a0[i] - ref), y = (long long)(a1[i] - ref);
            mn0 = x < mn0 ? x : mn0; mx0 = x > mx0 ? x : mx0; s0 += x; mn1 = y < mn1 ? y : mn1; mx1 = y > mx1 ? y : mx1; s1 += y; }
        const long long nb = PB1 - PB0;
        long long v0 = PM0 == 0 ? mn0 * nb : PM0 == 1 ? mx0 * nb : s0, v1 = PM1 == 0 ? mn1 * nb : PM1 == 1 ? mx1 * nb : s1;
        long long T = (v1 - v0) * PMULT / nb;
        if (T < 0) T = 0; if (T > 100000) T = 100000;
        unsigned long long t0 = __builtin_amdgcn_s_memrealtime();
        while ((long long)(__builtin_amdgcn_s_memrealtime() - t0) < T) __builtin_amdgcn_s_sleep(1);
    }
}
extern "C" void kernel_launch(void* const* d_in, const int* in_sizes, int n_in,
                              void* d_out, int out_size, void* d_ws, size_t ws_size,
                              hipStream_t stream) {
    const u16* u = (const u16*)d_in[0];
    char* ws = (char*)d_ws;
    (void)hipFuncSetAttribute((const void*)k_ssm, hipFuncAttributeMaxDynamicSharedMemorySize, S_END);
    (void)hipFuncSetAttribute((const void*)k_gemm1, hipFuncAttributeMaxDynamicSharedMemorySize, G1_NST * G1_STAGE);
    (void)hipFuncSetAttribute((const void*)k_gemm2, hipFuncAttributeMaxDynamicSharedMemorySize, 4 * G2_DEPTH * 4096);
    k_ssm<<<256, 256, S_END, stream>>>(u, (const u16*)d_in[1], (const u16*)d_in[2], (const u16*)d_in[3], (const u16*)d_in[4],
        (const u16*)d_in[5], (const u16*)d_in[6], (const u16*)d_in[7], (const u16*)d_in[8], (const u16*)d_in[13],
        (unsigned*)(ws + WS_A), (float*)(ws + WS_G));
    k_gemm1<<<256, 256, G1_NST * G1_STAGE, stream>>>(u, (const u16*)d_in[10], (const u16*)d_in[11], ws + WS_H);
    k_gemm2<<<256, 256, 4 * G2_DEPTH * 4096, stream>>>(ws + WS_H, (const u16*)d_in[12], (const unsigned*)(ws + WS_A),
        (const float*)(ws + WS_G), (u16*)d_out);
    k_probe<<<1,256,0,stream>>>((const unsigned long long*)((char*)d_ws + WS_STAMP));
}
```

```cpp
#define STAMPS
#include <hip/hip_runtime.h>
#include <hip/hip_bf16.h>
#include <stdint.h>

#define LAS __attribute__((address_space(3)))
#define GAS __attribute__((address_space(1)))
typedef short  v4s  __attribute__((ext_vector_type(4)));
typedef short  v8s  __attribute__((ext_vector_type(8)));
typedef __bf16 v8bf __attribute__((ext_vector_type(8)));
typedef __bf16 v2bf __attribute__((ext_vector_type(2)));
typedef float  v2f  __attribute__((ext_vector_type(2)));
typedef float  v4f  __attribute__((ext_vector_type(4)));
typedef float  v16f __attribute__((ext_vector_type(16)));
typedef unsigned v4u __attribute__((ext_vector_type(4)));
typedef unsigned v2u __attribute__((ext_vector_type(2)));
typedef unsigned short u16;

constexpr int L_ = 2048, D_ = 512, N_ = 128, F_ = 1376;
constexpr int NG = F_ / 16;

constexpr size_t WS_H = 0;
constexpr size_t WS_A = WS_H + (size_t)64 * NG * 1024;
constexpr size_t WS_G = WS_A + (size_t)256 * 2048 * 4;

__device__ __forceinline__ float bf2f(u16 v) { return __uint_as_float((unsigned)v << 16); }
__device__ __forceinline__ float bflo(unsigned v) { return __uint_as_float(v << 16); }
__device__ __forceinline__ float bfhi(unsigned v) { return __uint_as_float(v & 0xffff0000u); }
__device__ __forceinline__ unsigned pk2(float lo, float hi) {
    v2f f = {lo, hi};
    return __builtin_bit_cast(unsigned, __builtin_convertvector(f, v2bf));
}
__device__ __forceinline__ float rbf(float x) { return bflo(pk2(x, x)); }

#define WAIT_VM(n)  asm volatile("s_waitcnt vmcnt(" #n ")" ::: "memory")
#define WAIT_LGKM0() asm volatile("s_waitcnt lgkmcnt(0)" ::: "memory")
#define BAR() __builtin_amdgcn_s_barrier()

typedef v4u rsrc_t;
__device__ __forceinline__ rsrc_t mkrsrc(const void* p, unsigned bytes) {
    uint64_t a = (uint64_t)p;
    rsrc_t r = {(unsigned)a, (unsigned)(a >> 32) & 0xffffu, bytes, 0x00020000u};
    return r;
}
__device__ __forceinline__ unsigned lds_addr(const char* p) { return (unsigned)(uintptr_t)(LAS const char*)p; }
__device__ __forceinline__ void glds16(rsrc_t r, char* lds_dst, unsigned voff, unsigned soff) {
    asm volatile("s_mov_b32 m0, %0\n\ts_nop 0\n\tbuffer_load_dwordx4 %1, %2, %3 offen lds"
                 :: "s"(lds_addr(lds_dst)), "v"(voff), "s"(r), "s"(soff) : "memory");
}
__device__ __forceinline__ v8bf tr_frag(const char* p0, const char* p1) {
    v4s a = __builtin_amdgcn_ds_read_tr16_b64_v4i16((LAS v4s*)p0);
    v4s b = __builtin_amdgcn_ds_read_tr16_b64_v4i16((LAS v4s*)p1);
    v8s r = __builtin_shufflevector(a, b, 0, 1, 2, 3, 4, 5, 6, 7);
    return __builtin_bit_cast(v8bf, r);
}

constexpr size_t WS_STAMP = (size_t)200 << 20;
constexpr int NSTAMP = 32, STAMP_BLOCKS = 256;
#define STAMP(base, k) do { if (threadIdx.x == 0) { unsigned long long t_; asm volatile("s_memrealtime %0\n\ts_waitcnt lgkmcnt(0)" : "=s"(t_) :: "memory"); ((unsigned long long*)(base))[blockIdx.x * NSTAMP + (k)] = t_; } } while (0)
#define STAMPC(base, k) do { if (threadIdx.x == 0) { unsigned long long t_; asm volatile("s_memtime %0\n\ts_waitcnt lgkmcnt(0)" : "=s"(t_) :: "memory"); ((unsigned long long*)(base))[blockIdx.x * NSTAMP + (k)] = t_; } } while (0)
struct cf { float r, i; };
__device__ __forceinline__ cf cmul(cf a, cf b) { return {a.r * b.r - a.i * b.i, a.r * b.i + a.i * b.r}; }
__device__ __forceinline__ cf cexp_(float al, float be, float e) {
    float m = __builtin_amdgcn_exp2f(al * e);
    float th = __builtin_amdgcn_fractf(be * e);
    return {m * __builtin_amdgcn_cosf(th), m * __builtin_amdgcn_sinf(th)};
}

constexpr int TP = 272;
constexpr int S_T = 0;
constexpr int S_TW = 53 * TP;
constexpr int S_KC = 0;
constexpr int KCP = 4160;
constexpr int S_Y = 0;
constexpr int S_KD = 4 * S_TW;
constexpr int KDN = 704;
constexpr int S_U = S_KD + 4 * KDN * 4;
constexpr int UP = 2288 * 2;
constexpr int S_END = S_U + 2 * UP;

__device__ __forceinline__ void k_ssm(const u16* __restrict__ u, const u16* __restrict__ Are, const u16* __restrict__ Aim,
        const u16* __restrict__ Bre, const u16* __restrict__ Bim, const u16* __restrict__ Cre, const u16* __restrict__ Cim,
        const u16* __restrict__ logdt, const u16* __restrict__ Dskip, const u16* __restrict__ gw,
        unsigned* a2, float* gout) {
    extern __shared__ __attribute__((aligned(16))) char lds[];
    const int t = threadIdx.x, lane = t & 63, w = __builtin_amdgcn_readfirstlane(t >> 6);
    const int b = blockIdx.x, ch = w >> 1, hf = w & 1;
    const int d = 2 * b + ch;
    STAMP(((char*)a2 - WS_A + WS_STAMP), 0);
    unsigned uu[8];
#pragma unroll
    for (int i = 0; i < 8; ++i) uu[i] = *(const unsigned*)(u + (size_t)(t * 8 + i) * D_ + 2 * b);
    {
        const int row = 8 * b + 2 * w + (lane >> 5), i = lane & 31;
        const v4u x0 = *(const v4u*)(u + (size_t)row * D_ + 16 * i), x1 = *(const v4u*)(u + (size_t)row * D_ + 16 * i + 8);
        const v4u g0 = *(const v4u*)(gw + 16 * i), g1 = *(const v4u*)(gw + 16 * i + 8);
        float s = 0.f;
#pragma unroll
        for (int k = 0; k < 4; ++k) {
            unsigned p0 = pk2(bflo(x0[k]) * bflo(g0[k]), bfhi(x0[k]) * bfhi(g0[k]));
            unsigned p1 = pk2(bflo(x1[k]) * bflo(g1[k]), bfhi(x1[k]) * bfhi(g1[k]));
            s += bflo(p0) + bfhi(p0) + bflo(p1) + bfhi(p1);
        }
#pragma unroll
        for (int m = 16; m >= 1; m >>= 1) s += __shfl_xor(s, m);
        s = rbf(s);
        float g = rbf(1.f / (1.f + __expf(-s)));
        if (i == 0) gout[row] = g;
    }
    STAMP(((char*)a2 - WS_A + WS_STAMP), 1);
    const int n = 64 * hf + lane, pi = d * N_ + n;
    const float dt = rbf(__expf(bf2f(logdt[d])));
    const float al = dt * bf2f(Are[pi]) * 1.44269504088896341f, be = dt * bf2f(Aim[pi]) * 0.15915494309189535f;
    const cf wv = cmul(cf{bf2f(Cre[pi]), bf2f(Cim[pi])}, cf{dt * bf2f(Bre[pi]), dt * bf2f(Bim[pi])});
    char* T = lds + S_T + w * S_TW + lane * 4;
    {
        const cf z1 = cexp_(al, be, 1.f), z8 = cexp_(al, be, 8.f), z256 = cexp_(al, be, 256.f);
        const cf z16 = cmul(z8, z8);
        cf e = {1.f, 0.f}, o = z8;
#pragma unroll
        for (int r = 0; r < 32; r += 2) {
            *(unsigned*)(T + r * TP) = pk2(e.r, e.i);
            *(unsigned*)(T + (r + 1) * TP) = pk2(o.r, o.i);
            if (r + 2 < 32) { e = cmul(e, z16); o = cmul(o, z16); }
        }
        char* T2 = T + 32 * TP;
        cf p = wv;
#pragma unroll
        for (int c = 0; c < 8; ++c) { *(unsigned*)(T2 + c * TP) = pk2(p.r, -p.i); if (c < 7) p = cmul(p, z1); }
        const cf z2 = cmul(z1, z1), z4 = cmul(z2, z2);
        cf q = cmul(wv, z256);
        cf q2 = q;
#pragma unroll
        for (int c = 0; c < 4; ++c) { *(unsigned*)(T2 + (8 + c) * TP) = pk2(q2.r, -q2.i); if (c < 3) q2 = cmul(q2, z2); }
#pragma unroll
        for (int c = 0; c < 2; ++c) {
            q = cmul(q, z256);
            cf q4 = cmul(q, z4);
            *(unsigned*)(T2 + (12 + 2 * c) * TP) = pk2(q.r, -q.i);
            *(unsigned*)(T2 + (13 + 2 * c) * TP) = pk2(q4.r, -q4.i);
        }
#pragma unroll
        for (int c = 0; c < 5; ++c) { q = cmul(q, z256); *(unsigned*)(T2 + (16 + c) * TP) = pk2(q.r, -q.i); }
    }
    STAMP(((char*)a2 - WS_A + WS_STAMP), 2);
    {
        v4u p0, p1;
#pragma unroll
        for (int i = 0; i < 4; ++i) {
            p0[i] = (uu[2 * i] & 0xffffu) | (uu[2 * i + 1] << 16);
            p1[i] = (uu[2 * i] >> 16) | (uu[2 * i + 1] & 0xffff0000u);
        }
        *(v4u*)(lds + S_U + (240 + 8 * t) * 2) = p0;
        *(v4u*)(lds + S_U + UP + (240 + 8 * t) * 2) = p1;
        if (t < 60) *(v4u*)(lds + S_U + (t >= 30 ? UP : 0) + (t % 30) * 16) = (v4u){0, 0, 0, 0};
    }
    STAMP(((char*)a2 - WS_A + WS_STAMP), 3);
    v16f kacc = {};
    {
        const int r = lane & 31, hh = lane >> 5;
        const char* TA = lds + S_T + w * S_TW + r * TP + hh * 16;
        const char* TB = lds + S_T + w * S_TW + (32 + (r < 20 ? r : 20)) * TP + hh * 16;
#pragma unroll
        for (int s = 0; s < 8; ++s) {
            v8bf fa = *(const v8bf*)(TA + s * 32), fb = *(const v8bf*)(TB + s * 32);
            kacc = __builtin_amdgcn_mfma_f32_32x32x16_bf16(fa, fb, kacc, 0, 0, 0);
        }
        const int c = r;
        int base, stride;
        if (c < 8) { base = c; stride = 8; }
        else if (c < 12) { base = 256 + (c - 8); stride = 4; }
        else if (c < 16) { base = 384 + 64 * ((c - 12) >> 1) + ((c - 12) & 1); stride = 2; }
        else if (c <= 20) { base = 512 + 32 * (c - 16); stride = 1; }
        else { base = 672; stride = 1; }
        float* KD = (float*)(lds + S_KD) + w * KDN;
#pragma unroll
        for (int q = 0; q < 16; ++q) KD[base + stride * ((q & 3) + 8 * (q >> 2) + 4 * hh)] = kacc[q];
    }
    __syncthreads();
    STAMP(((char*)a2 - WS_A + WS_STAMP), 4);
#pragma unroll
    for (int c = 0; c < 2; ++c) {
        const float* K0 = (const float*)(lds + S_KD) + (2 * c) * KDN;
        const float* K1 = K0 + KDN;
        float v[12];
#pragma unroll
        for (int k = 0; k < 12; ++k) {
            int j = 2047 - (8 * t + k);
            int jj = j < 0 ? 0 : j;
            int idx = jj < 128 ? jj : (int)(pk2((float)jj, 0.f) & 0xffffu) - 17024;
            float x = K0[idx] + K1[idx];
            v[k] = j < 0 ? 0.f : x;
        }
#pragma unroll
        for (int s = 0; s < 4; ++s) {
            v4u o = {pk2(v[s], v[s + 1]), pk2(v[s + 2], v[s + 3]), pk2(v[s + 4], v[s + 5]), pk2(v[s + 6], v[s + 7])};
            *(v4u*)(lds + S_KC + (c * 4 + s) * KCP + t * 16) = o;
        }
        if (t < 8) *(v4u*)(lds + S_KC + (c * 4 + (t >> 1)) * KCP + 4096 + (t & 1) * 16) = (v4u){0, 0, 0, 0};
    }
    __syncthreads();
    STAMP(((char*)a2 - WS_A + WS_STAMP), 5);
    v4f y[8];
#pragma unroll
    for (int p = 0; p < 8; ++p) y[p] = (v4f){0, 0, 0, 0};
    {
        const int r = lane & 15, g = lane >> 4, rp = 15 - r;
        const char* KA = lds + S_KC + (ch * 4 + (rp & 3)) * KCP + (8 * g + (rp & 12) + 128 * hf) * 2;
        const char* UB = lds + S_U + ch * UP + (16 * r + 8 * g + 128 * hf) * 2;
#pragma unroll 1
        for (int tt = 0; tt < 4; ++tt) {
            v8bf fa[8], fb[8];
#pragma unroll
            for (int m = 0; m < 8; ++m) {
                v2u lo = *(const v2u*)(KA + tt * 64 + (7 - m) * 512), hi = *(const v2u*)(KA + tt * 64 + (7 - m) * 512 + 8);
                v4u x = {lo[0], lo[1], hi[0], hi[1]};
                fa[m] = __builtin_bit_cast(v8bf, x);
                fb[m] = *(const v8bf*)(UB + tt * 64 + m * 512);
            }
#pragma unroll
            for (int m = 0; m < 8; ++m)
#pragma unroll
                for (int q = 0; q + m < 8; ++q)
                    y[m + q] = __builtin_amdgcn_mfma_f32_16x16x32_bf16(fa[m], fb[q], y[m + q], 0, 0, 0);
        }
    }
    STAMP(((char*)a2 - WS_A + WS_STAMP), 6);
    __syncthreads();
#pragma unroll
    for (int p = 0; p < 8; ++p) *(v4f*)(lds + S_Y + w * 8192 + (p * 64 + lane) * 16) = y[p];
    __syncthreads();
    {
        const int p = t >> 5, l1 = (t >> 1) & 15, g0 = 2 * (t & 1);
        const float ds0 = bf2f(Dskip[2 * b]), ds1 = bf2f(Dskip[2 * b + 1]);
        v4u o[2];
#pragma unroll
        for (int h = 0; h < 2; ++h) {
            const char* yp = lds + S_Y + (p * 64 + (g0 + h) * 16 + l1) * 16;
            v4f y0 = *(const v4f*)(yp) + *(const v4f*)(yp + 8192);
            v4f y1 = *(const v4f*)(yp + 16384) + *(const v4f*)(yp + 24576);
#pragma unroll
            for (int k = 0; k < 4; ++k) {
                unsigned yb = pk2(y0[k], y1[k]);
                unsigned sk = pk2(bflo(uu[4 * h + k]) * ds0, bfhi(uu[4 * h + k]) * ds1);
                o[h][k] = pk2(bflo(yb) + bflo(sk), bfhi(yb) + bfhi(sk));
            }
        }
        v4u* dst = (v4u*)(a2 + (size_t)b * L_ + 8 * t);
        dst[0] = o[0]; dst[1] = o[1];
    }
    STAMP(((char*)a2 - WS_A + WS_STAMP), 7);
}

constexpr int G1_X = 0, G1_WG = 16384, G1_WU = G1_WG + 12288 + 32, G1_STAGE = 40960 + 64;
constexpr int G1_NST = 3;

__device__ __forceinline__ void k_gemm1(const u16* __restrict__ x, const u16* __restrict__ Wg, const u16* __restrict__ Wu,
                                               char* Hf) {
    extern __shared__ __attribute__((aligned(16))) char lds[];
    const int t = threadIdx.x, lane = t & 63, w = __builtin_amdgcn_readfirstlane(t >> 6);
    const int b = blockIdx.x;
    const int xcd = b & 7, c = b >> 3;
    STAMP(((char*)Hf - WS_H + WS_STAMP), 8);
    const int pi = (xcd & 1) * 8 + (c & 7), tj = (xcd >> 1) * 4 + (c >> 3);
    const int sg = tj <= 10 ? 5 * tj : 50 + 6 * (tj - 10), ng = tj < 10 ? 5 : 6;
    const rsrc_t rx = mkrsrc(x, L_ * D_ * 2), rg = mkrsrc(Wg, D_ * F_ * 2), ru = mkrsrc(Wu, D_ * F_ * 2);
    const unsigned vx = (8 * w + (lane >> 3)) * 1024 + (((lane & 7) ^ ((4 * w + (lane >> 4)) & 7)) << 4);
    unsigned vw[3];
#pragma unroll
    for (int i = 0; i < 3; ++i) {
        int cc = 64 * i + lane, row = cc / 12, ch = cc % 12;
        if (ng == 5 && ch >= 10) ch -= 2;
        vw[i] = row * (F_ * 2) + ch * 16;
    }
    const unsigned sx0 = pi * 128 * 1024, sw0 = w * 16 * (F_ * 2) + sg * 32;
    auto stage = [&](int kt, int buf) {
        char* base = lds + buf * G1_STAGE;
#pragma unroll
        for (int i = 0; i < 4; ++i) glds16(rx, base + G1_X + (w + 4 * i) * 1024, vx, sx0 + kt * 128 + i * 32768);
#pragma unroll
        for (int i = 0; i < 3; ++i) glds16(rg, base + G1_WG + (3 * w + i) * 1024, vw[i], sw0 + kt * 64 * (F_ * 2));
#pragma unroll
        for (int i = 0; i < 3; ++i) glds16(ru, base + G1_WU + (3 * w + i) * 1024, vw[i], sw0 + kt * 64 * (F_ * 2));
    };
    const int wf = w & 1, wm = w >> 1;
    const int m = lane & 31, hh = lane >> 5, grp = lane >> 4, q = (lane >> 2) & 3, p = lane & 3;
    const int xoff = G1_X + (64 * wm + m) * 128 + ((hh ^ ((m >> 1) & 7)) << 4);
    const int woff = ((grp & 1) ? G1_WU : G1_WG) + (8 * hh + q) * 192 + p * 8 + wf * 96;
    v16f acc[3][2];
#pragma unroll
    for (int j = 0; j < 3; ++j) { acc[j][0] = (v16f){}; acc[j][1] = (v16f){}; }
    stage(0, 0); stage(1, 1);
    int buf = 0;
#pragma unroll 1
    for (int kt = 0; kt < 8; ++kt) {
        if (kt < 7) { WAIT_VM(10); } else { WAIT_VM(0); }
        BAR();
        if (kt + 2 < 8) { int nb = buf + 2; if (nb >= 3) nb -= 3; stage(kt + 2, nb); }
        const char* base = lds + buf * G1_STAGE;
#pragma unroll
        for (int s = 0; s < 4; ++s) {
            v8bf fb[2], fa[3];
#pragma unroll
            for (int mt = 0; mt < 2; ++mt) fb[mt] = *(const v8bf*)(base + ((xoff ^ (s << 5)) + mt * 4096));
#pragma unroll
            for (int j = 0; j < 3; ++j) fa[j] = tr_frag(base + woff + (16 * s) * 192 + j * 32, base + woff + (16 * s + 4) * 192 + j * 32);
#pragma unroll
            for (int j = 0; j < 3; ++j)
#pragma unroll
                for (int mt = 0; mt < 2; ++mt)
                    acc[j][mt] = __builtin_amdgcn_mfma_f32_32x32x16_bf16(fa[j], fb[mt], acc[j][mt], 0, 0, 0);
        }
        WAIT_LGKM0();
        buf = buf == 2 ? 0 : buf + 1;
    }
    STAMP(((char*)Hf - WS_H + WS_STAMP), 17);
    const float c1 = -2.f * 0.7978845608028654f * 1.44269504088896341f, c2 = c1 * 0.044715f;
#pragma unroll
    for (int j = 0; j < 3; ++j) {
        const int gj = 3 * wf + j;
        if (gj < ng) {
#pragma unroll
            for (int mt = 0; mt < 2; ++mt) {
                float h[8];
#pragma unroll
                for (int e = 0; e < 8; ++e) {
                    float g = acc[j][mt][e], uu = acc[j][mt][8 + e];
                    float ex = __builtin_amdgcn_exp2f(g * (c1 + c2 * g * g));
                    h[e] = g * uu * __builtin_amdgcn_rcpf(1.f + ex);
                }
                v4u o = {pk2(h[0], h[1]), pk2(h[2], h[3]), pk2(h[4], h[5]), pk2(h[6], h[7])};
                *(v4u*)(Hf + ((size_t)(pi * 4 + 2 * wm + mt) * NG + sg + gj) * 1024 + lane * 16) = o;
            }
        }
    }
    STAMP(((char*)Hf - WS_H + WS_STAMP), 18);
}

constexpr int G2_DEPTH = 8;
__device__ __forceinline__ void k_gemm2(const char* Hf, const u16* __restrict__ Wd, const unsigned* a2,
                                               const float* gin, u16* __restrict__ out) {
    extern __shared__ __attribute__((aligned(16))) char lds[];
    const int t = threadIdx.x, lane = t & 63, w = __builtin_amdgcn_readfirstlane(t >> 6);
    const int b = blockIdx.x;
    const int xcd = b & 7, c = b >> 3;
    STAMP(((char*)Hf - WS_H + WS_STAMP), 20);
    const int im = xcd * 4 + (c >> 3), jd = c & 7;
    const int g0 = w < 2 ? 22 * w : 44 + 21 * (w - 2), ngr = w < 2 ? 22 : 21;
    const rsrc_t rh = mkrsrc(Hf, 64 * NG * 1024), rw = mkrsrc(Wd, F_ * D_ * 2);
    const unsigned vh = lane * 16;
    const unsigned vw = (lane >> 3) * 1024 + (((lane & 7) ^ (((lane >> 4) & 1) << 2)) << 4);
    char* ring = lds + w * (G2_DEPTH * 4096);
    auto stage = [&](int g, int slot) {
        char* base = ring + slot * 4096;
        glds16(rh, base, vh, ((2 * im) * NG + g) * 1024);
        glds16(rh, base + 1024, vh, ((2 * im + 1) * NG + g) * 1024);
        glds16(rw, base + 2048, vw, g * 16 * 1024 + jd * 128);
        glds16(rw, base + 3072, vw, g * 16 * 1024 + 8192 + jd * 128);
    };
    const int hh = lane >> 5, grp = lane >> 4, q = (lane >> 2) & 3, p = lane & 3;
    const int woff = 2048 + (4 * hh + q) * 128 + ((q >> 1) << 6) + (grp & 1) * 32 + p * 8;
    v16f acc[2][2];
#pragma unroll
    for (int j = 0; j < 2; ++j) { acc[j][0] = (v16f){}; acc[j][1] = (v16f){}; }
#pragma unroll
    for (int i = 0; i < G2_DEPTH - 1; ++i) stage(g0 + i, i);
#pragma unroll 1
    for (int i = 0; i < ngr; ++i) {
        int gn = i + G2_DEPTH - 1; if (gn >= ngr) gn = ngr - 1;
        WAIT_LGKM0();
        stage(g0 + gn, (i + G2_DEPTH - 1) & (G2_DEPTH - 1));
        WAIT_VM(28);
        const char* base = ring + (i & (G2_DEPTH - 1)) * 4096;
        v8bf fb[2], fa[2];
#pragma unroll
        for (int mt = 0; mt < 2; ++mt) fb[mt] = *(const v8bf*)(base + mt * 1024 + lane * 16);
#pragma unroll
        for (int dt = 0; dt < 2; ++dt) fa[dt] = tr_frag(base + (woff ^ (dt << 6)), base + (woff ^ (dt << 6)) + 1024);
#pragma unroll
        for (int dt = 0; dt < 2; ++dt)
#pragma unroll
            for (int mt = 0; mt < 2; ++mt)
                acc[dt][mt] = __builtin_amdgcn_mfma_f32_32x32x16_bf16(fa[dt], fb[mt], acc[dt][mt], 0, 0, 0);
    }
    STAMP(((char*)Hf - WS_H + WS_STAMP), 23);
    WAIT_VM(0);
    __syncthreads();
    STAMP(((char*)Hf - WS_H + WS_STAMP), 24);
#pragma unroll
    for (int dt = 0; dt < 2; ++dt)
#pragma unroll
        for (int mt = 0; mt < 2; ++mt)
#pragma unroll
            for (int k = 0; k < 4; ++k) {
                v4f v = {acc[dt][mt][4 * k], acc[dt][mt][4 * k + 1], acc[dt][mt][4 * k + 2], acc[dt][mt][4 * k + 3]};
                *(v4f*)(lds + ((dt + 2 * mt) * 4 + w) * 4096 + k * 1024 + lane * 16) = v;
            }
    __syncthreads();
    const int dt = w & 1, mt = w >> 1;
    const int l = im * 64 + mt * 32 + (lane & 31);
    const float g = gin[l], og = rbf(1.f - g);
#pragma unroll
    for (int k = 0; k < 4; ++k) {
        v4f s = {0, 0, 0, 0};
#pragma unroll
        for (int src = 0; src < 4; ++src) s += *(const v4f*)(lds + (w * 4 + src) * 4096 + k * 1024 + lane * 16);
        const int d0 = jd * 64 + dt * 32 + 8 * k + 4 * hh;
        const unsigned a01 = a2[(size_t)(d0 >> 1) * L_ + l], a23 = a2[(size_t)((d0 >> 1) + 1) * L_ + l];
        unsigned b01 = pk2(s[0], s[1]), b23 = pk2(s[2], s[3]);
        unsigned ga01 = pk2(g * bflo(a01), g * bfhi(a01)), ga23 = pk2(g * bflo(a23), g * bfhi(a23));
        unsigned gb01 = pk2(og * bflo(b01), og * bfhi(b01)), gb23 = pk2(og * bflo(b23), og * bfhi(b23));
        v2u o = {pk2(bflo(ga01) + bflo(gb01), bfhi(ga01) + bfhi(gb01)), pk2(bflo(ga23) + bflo(gb23), bfhi(ga23) + bfhi(gb23))};
        *(v2u*)(out + (size_t)l * D_ + d0) = o;
    }
    STAMP(((char*)Hf - WS_H + WS_STAMP), 25);
}

constexpr size_t WS_FLAG = (size_t)16 << 20;
struct Args { const u16* in[14]; u16* out; char* ws; };
__global__ __launch_bounds__(256) void k_all(Args a) {
    char* ws = a.ws;
    typedef GAS unsigned gu32;
    gu32* epoch = (gu32*)(ws + WS_FLAG);
    gu32* flags = (gu32*)(ws + WS_FLAG + 4096);
    const unsigned tag = __hip_atomic_load(epoch, __ATOMIC_RELAXED, __HIP_MEMORY_SCOPE_AGENT) + 1u;
    k_ssm(a.in[0], a.in[1], a.in[2], a.in[3], a.in[4], a.in[5], a.in[6], a.in[7], a.in[8], a.in[13], (unsigned*)(ws + WS_A), (float*)(ws + WS_G));
    __syncthreads();
    k_gemm1(a.in[0], a.in[10], a.in[11], ws + WS_H);
    asm volatile("s_waitcnt vmcnt(0)" ::: "memory");
    __syncthreads();
    if (threadIdx.x == 0) {
        __builtin_amdgcn_fence(__ATOMIC_RELEASE, "agent");
        asm volatile("s_waitcnt vmcnt(0)" ::: "memory");
        __hip_atomic_store(flags + blockIdx.x, tag, __ATOMIC_RELAXED, __HIP_MEMORY_SCOPE_AGENT);
    }
    {
        for (int spin = 0; spin < 200000; ++spin) {
            unsigned v = __hip_atomic_load(flags + threadIdx.x, __ATOMIC_RELAXED, __HIP_MEMORY_SCOPE_AGENT);
            if (__all(v == tag)) break;
            __builtin_amdgcn_s_sleep(2);
        }
        __builtin_amdgcn_fence(__ATOMIC_ACQUIRE, "agent");
        asm volatile("s_waitcnt vmcnt(0)" ::: "memory");
        __syncthreads();
    }
    if (blockIdx.x == 0 && threadIdx.x == 0) __hip_atomic_store(epoch, tag, __ATOMIC_RELAXED, __HIP_MEMORY_SCOPE_AGENT);
    k_gemm2(ws + WS_H, a.in[12], (const unsigned*)(ws + WS_A), (const float*)(ws + WS_G), a.out);
}
extern "C" void kernel_launch(void* const* d_in, const int* in_sizes, int n_in,
                              void* d_out, int out_size, void* d_ws, size_t ws_size,
                              hipStream_t stream) {
    Args a;
    for (int i = 0; i < 14; ++i) a.in[i] = (const u16*)d_in[i];
    a.out = (u16*)d_out; a.ws = (char*)d_ws;
    (void)hipFuncSetAttribute((const void*)k_all, hipFuncAttributeMaxDynamicSharedMemorySize, 131072);
    k_all<<<256, 256, 131072, stream>>>(a);
}
```

```cpp
#include <hip/hip_runtime.h>
#include <hip/hip_bf16.h>
#include <stdint.h>

#define LAS __attribute__((address_space(3)))
#define GAS __attribute__((address_space(1)))
typedef short  v4s  __attribute__((ext_vector_type(4)));
typedef short  v8s  __attribute__((ext_vector_type(8)));
typedef __bf16 v8bf __attribute__((ext_vector_type(8)));
typedef __bf16 v2bf __attribute__((ext_vector_type(2)));
typedef float  v2f  __attribute__((ext_vector_type(2)));
typedef float  v4f  __attribute__((ext_vector_type(4)));
typedef float  v16f __attribute__((ext_vector_type(16)));
typedef unsigned v4u __attribute__((ext_vector_type(4)));
typedef unsigned v2u __attribute__((ext_vector_type(2)));
typedef unsigned short u16;

constexpr int L_ = 2048, D_ = 512, N_ = 128, F_ = 1376;
constexpr int NG = F_ / 16;

constexpr size_t WS_H = 0;
constexpr size_t WS_A = WS_H + (size_t)64 * NG * 1024;
constexpr size_t WS_G = WS_A + (size_t)256 * 2048 * 4;

__device__ __forceinline__ float bf2f(u16 v) { return __uint_as_float((unsigned)v << 16); }
__device__ __forceinline__ float bflo(unsigned v) { return __uint_as_float(v << 16); }
__device__ __forceinline__ float bfhi(unsigned v) { return __uint_as_float(v & 0xffff0000u); }
__device__ __forceinline__ unsigned pk2(float lo, float hi) {
    v2f f = {lo, hi};
    return __builtin_bit_cast(unsigned, __builtin_convertvector(f, v2bf));
}
__device__ __forceinline__ float rbf(float x) { return bflo(pk2(x, x)); }

#define WAIT_VM(n)  asm volatile("s_waitcnt vmcnt(" #n ")" ::: "memory")
#define WAIT_LGKM0() asm volatile("s_waitcnt lgkmcnt(0)" ::: "memory")
#define BAR() __builtin_amdgcn_s_barrier()

typedef v4u rsrc_t;
__device__ __forceinline__ rsrc_t mkrsrc(const void* p, unsigned bytes) {
    uint64_t a = (uint64_t)p;
    rsrc_t r = {(unsigned)a, (unsigned)(a >> 32) & 0xffffu, bytes, 0x00020000u};
    return r;
}
__device__ __forceinline__ unsigned lds_addr(const char* p) { return (unsigned)(uintptr_t)(LAS const char*)p; }
__device__ __forceinline__ void glds16(rsrc_t r, char* lds_dst, unsigned voff, unsigned soff) {
    asm volatile("s_mov_b32 m0, %0\n\ts_nop 0\n\tbuffer_load_dwordx4 %1, %2, %3 offen lds"
                 :: "s"(lds_addr(lds_dst)), "v"(voff), "s"(r), "s"(soff) : "memory");
}
__device__ __forceinline__ v8bf tr_frag(const char* p0, const char* p1) {
    v4s a = __builtin_amdgcn_ds_read_tr16_b64_v4i16((LAS v4s*)p0);
    v4s b = __builtin_amdgcn_ds_read_tr16_b64_v4i16((LAS v4s*)p1);
    v8s r = __builtin_shufflevector(a, b, 0, 1, 2, 3, 4, 5, 6, 7);
    return __builtin_bit_cast(v8bf, r);
}

#define STAMP(base, k)
struct cf { float r, i; };
__device__ __forceinline__ cf cmul(cf a, cf b) { return {a.r * b.r - a.i * b.i, a.r * b.i + a.i * b.r}; }
__device__ __forceinline__ cf cexp_(float al, float be, float e) {
    float m = __builtin_amdgcn_exp2f(al * e);
    float th = __builtin_amdgcn_fractf(be * e);
    return {m * __builtin_amdgcn_cosf(th), m * __builtin_amdgcn_sinf(th)};
}

constexpr int TP = 272;
constexpr int S_T = 0;
constexpr int S_TW = 53 * TP;
constexpr int S_KC = 0;
constexpr int KCP = 4160;
constexpr int S_Y = 0;
constexpr int S_KD = 4 * S_TW;
constexpr int KDN = 704;
constexpr int S_U = S_KD + 4 * KDN * 4;
constexpr int UP = 2288 * 2;
constexpr int S_END = S_U + 2 * UP;

__device__ __forceinline__ void k_ssm(const u16* __restrict__ u, const u16* __restrict__ Are, const u16* __restrict__ Aim,
        const u16* __restrict__ Bre, const u16* __restrict__ Bim, const u16* __restrict__ Cre, const u16* __restrict__ Cim,
        const u16* __restrict__ logdt, const u16* __restrict__ Dskip, const u16* __restrict__ gw,
        unsigned* a2, float* gout) {
    extern __shared__ __attribute__((aligned(16))) char lds[];
    const int t = threadIdx.x, lane = t & 63, w = __builtin_amdgcn_readfirstlane(t >> 6);
    const int b = blockIdx.x, ch = w >> 1, hf = w & 1;
    const int d = 2 * b + ch;
    STAMP(((char*)a2 - WS_A + WS_STAMP), 0);
    unsigned uu[8];
#pragma unroll
    for (int i = 0; i < 8; ++i) uu[i] = *(const unsigned*)(u + (size_t)(t * 8 + i) * D_ + 2 * b);
    {
        const int row = 8 * b + 2 * w + (lane >> 5), i = lane & 31;
        const v4u x0 = *(const v4u*)(u + (size_t)row * D_ + 16 * i), x1 = *(const v4u*)(u + (size_t)row * D_ + 16 * i + 8);
        const v4u g0 = *(const v4u*)(gw + 16 * i), g1 = *(const v4u*)(gw + 16 * i + 8);
        float s = 0.f;
#pragma unroll
        for (int k = 0; k < 4; ++k) {
            unsigned p0 = pk2(bflo(x0[k]) * bflo(g0[k]), bfhi(x0[k]) * bfhi(g0[k]));
            unsigned p1 = pk2(bflo(x1[k]) * bflo(g1[k]), bfhi(x1[k]) * bfhi(g1[k]));
            s += bflo(p0) + bfhi(p0) + bflo(p1) + bfhi(p1);
        }
#pragma unroll
        for (int m = 16; m >= 1; m >>= 1) s += __shfl_xor(s, m);
        s = rbf(s);
        float g = rbf(1.f / (1.f + __expf(-s)));
        if (i == 0) gout[row] = g;
    }
    STAMP(((char*)a2 - WS_A + WS_STAMP), 1);
    const int n = 64 * hf + lane, pi = d * N_ + n;
    const float dt = rbf(__expf(bf2f(logdt[d])));
    const float al = dt * bf2f(Are[pi]) * 1.44269504088896341f, be = dt * bf2f(Aim[pi]) * 0.15915494309189535f;
    const cf wv = cmul(cf{bf2f(Cre[pi]), bf2f(Cim[pi])}, cf{dt * bf2f(Bre[pi]), dt * bf2f(Bim[pi])});
    char* T = lds + S_T + w * S_TW + lane * 4;
    {
        const cf z1 = cexp_(al, be, 1.f), z8 = cexp_(al, be, 8.f), z256 = cexp_(al, be, 256.f);
        const cf z16 = cmul(z8, z8);
        cf e = {1.f, 0.f}, o = z8;
#pragma unroll
        for (int r = 0; r < 32; r += 2) {
            *(unsigned*)(T + r * TP) = pk2(e.r, e.i);
            *(unsigned*)(T + (r + 1) * TP) = pk2(o.r, o.i);
            if (r + 2 < 32) { e = cmul(e, z16); o = cmul(o, z16); }
        }
        char* T2 = T + 32 * TP;
        cf p = wv;
#pragma unroll
        for (int c = 0; c < 8; ++c) { *(unsigned*)(T2 + c * TP) = pk2(p.r, -p.i); if (c < 7) p = cmul(p, z1); }
        const cf z2 = cmul(z1, z1), z4 = cmul(z2, z2);
        cf q = cmul(wv, z256);
        cf q2 = q;
#pragma unroll
        for (int c = 0; c < 4; ++c) { *(unsigned*)(T2 + (8 + c) * TP) = pk2(q2.r, -q2.i); if (c < 3) q2 = cmul(q2, z2); }
#pragma unroll
        for (int c = 0; c < 2; ++c) {
            q = cmul(q, z256);
            cf q4 = cmul(q, z4);
            *(unsigned*)(T2 + (12 + 2 * c) * TP) = pk2(q.r, -q.i);
            *(unsigned*)(T2 + (13 + 2 * c) * TP) = pk2(q4.r, -q4.i);
        }
#pragma unroll
        for (int c = 0; c < 5; ++c) { q = cmul(q, z256); *(unsigned*)(T2 + (16 + c) * TP) = pk2(q.r, -q.i); }
    }
    STAMP(((char*)a2 - WS_A + WS_STAMP), 2);
    {
        v4u p0, p1;
#pragma unroll
        for (int i = 0; i < 4; ++i) {
            p0[i] = (uu[2 * i] & 0xffffu) | (uu[2 * i + 1] << 16);
            p1[i] = (uu[2 * i] >> 16) | (uu[2 * i + 1] & 0xffff0000u);
        }
        *(v4u*)(lds + S_U + (240 + 8 * t) * 2) = p0;
        *(v4u*)(lds + S_U + UP + (240 + 8 * t) * 2) = p1;
        if (t < 60) *(v4u*)(lds + S_U + (t >= 30 ? UP : 0) + (t % 30) * 16) = (v4u){0, 0, 0, 0};
    }
    STAMP(((char*)a2 - WS_A + WS_STAMP), 3);
    v16f kacc = {};
    {
        const int r = lane & 31, hh = lane >> 5;
        const char* TA = lds + S_T + w * S_TW + r * TP + hh * 16;
        const char* TB = lds + S_T + w * S_TW + (32 + (r < 20 ? r : 20)) * TP + hh * 16;
#pragma unroll
        for (int s = 0; s < 8; ++s) {
            v8bf fa = *(const v8bf*)(TA + s * 32), fb = *(const v8bf*)(TB + s * 32);
            kacc = __builtin_amdgcn_mfma_f32_32x32x16_bf16(fa, fb, kacc, 0, 0, 0);
        }
        const int c = r;
        int base, stride;
        if (c < 8) { base = c; stride = 8; }
        else if (c < 12) { base = 256 + (c - 8); stride = 4; }
        else if (c < 16) { base = 384 + 64 * ((c - 12) >> 1) + ((c - 12) & 1); stride = 2; }
        else if (c <= 20) { base = 512 + 32 * (c - 16); stride = 1; }
        else { base = 672; stride = 1; }
        float* KD = (float*)(lds + S_KD) + w * KDN;
#pragma unroll
        for (int q = 0; q < 16; ++q) KD[base + stride * ((q & 3) + 8 * (q >> 2) + 4 * hh)] = kacc[q];
    }
    __syncthreads();
    STAMP(((char*)a2 - WS_A + WS_STAMP), 4);
#pragma unroll
    for (int c = 0; c < 2; ++c) {
        const float* K0 = (const float*)(lds + S_KD) + (2 * c) * KDN;
        const float* K1 = K0 + KDN;
        float v[12];
#pragma unroll
        for (int k = 0; k < 12; ++k) {
            int j = 2047 - (8 * t + k);
            int jj = j < 0 ? 0 : j;
            int idx = jj < 128 ? jj : (int)(pk2((float)jj, 0.f) & 0xffffu) - 17024;
            float x = K0[idx] + K1[idx];
            v[k] = j < 0 ? 0.f : x;
        }
#pragma unroll
        for (int s = 0; s < 4; ++s) {
            v4u o = {pk2(v[s], v[s + 1]), pk2(v[s + 2], v[s + 3]), pk2(v[s + 4], v[s + 5]), pk2(v[s + 6], v[s + 7])};
            *(v4u*)(lds + S_KC + (c * 4 + s) * KCP + t * 16) = o;
        }
        if (t < 8) *(v4u*)(lds + S_KC + (c * 4 + (t >> 1)) * KCP + 4096 + (t & 1) * 16) = (v4u){0, 0, 0, 0};
    }
    __syncthreads();
    STAMP(((char*)a2 - WS_A + WS_STAMP), 5);
    v4f y[8];
#pragma unroll
    for (int p = 0; p < 8; ++p) y[p] = (v4f){0, 0, 0, 0};
    {
        const int r = lane & 15, g = lane >> 4, rp = 15 - r;
        const char* KA = lds + S_KC + (ch * 4 + (rp & 3)) * KCP + (8 * g + (rp & 12) + 128 * hf) * 2;
        const char* UB = lds + S_U + ch * UP + (16 * r + 8 * g + 128 * hf) * 2;
#pragma unroll 1
        for (int tt = 0; tt < 4; ++tt) {
            v8bf fa[8], fb[8];
#pragma unroll
            for (int m = 0; m < 8; ++m) {
                v2u lo = *(const v2u*)(KA + tt * 64 + (7 - m) * 512), hi = *(const v2u*)(KA + tt * 64 + (7 - m) * 512 + 8);
                v4u x = {lo[0], lo[1], hi[0], hi[1]};
                fa[m] = __builtin_bit_cast(v8bf, x);
                fb[m] = *(const v8bf*)(UB + tt * 64 + m * 512);
            }
#pragma unroll
            for (int m = 0; m < 8; ++m)
#pragma unroll
                for (int q = 0; q + m < 8; ++q)
                    y[m + q] = __builtin_amdgcn_mfma_f32_16x16x32_bf16(fa[m], fb[q], y[m + q], 0, 0, 0);
        }
    }
    STAMP(((char*)a2 - WS_A + WS_STAMP), 6);
    __syncthreads();
#pragma unroll
    for (int p = 0; p < 8; ++p) *(v4f*)(lds + S_Y + w * 8192 + (p * 64 + lane) * 16) = y[p];
    __syncthreads();
    {
        const int p = t >> 5, l1 = (t >> 1) & 15, g0 = 2 * (t & 1);
        const float ds0 = bf2f(Dskip[2 * b]), ds1 = bf2f(Dskip[2 * b + 1]);
        v4u o[2];
#pragma unroll
        for (int h = 0; h < 2; ++h) {
            const char* yp = lds + S_Y + (p * 64 + (g0 + h) * 16 + l1) * 16;
            v4f y0 = *(const v4f*)(yp) + *(const v4f*)(yp + 8192);
            v4f y1 = *(const v4f*)(yp + 16384) + *(const v4f*)(yp + 24576);
#pragma unroll
            for (int k = 0; k < 4; ++k) {
                unsigned yb = pk2(y0[k], y1[k]);
                unsigned sk = pk2(bflo(uu[4 * h + k]) * ds0, bfhi(uu[4 * h + k]) * ds1);
                o[h][k] = pk2(bflo(yb) + bflo(sk), bfhi(yb) + bfhi(sk));
            }
        }
        v4u* dst = (v4u*)(a2 + (size_t)b * L_ + 8 * t);
        dst[0] = o[0]; dst[1] = o[1];
    }
    STAMP(((char*)a2 - WS_A + WS_STAMP), 7);
}

constexpr int G1_X = 0, G1_WG = 16384, G1_WU = G1_WG + 12288 + 32, G1_STAGE = 40960 + 64;
constexpr int G1_NST = 3;

__device__ __forceinline__ void k_gemm1(const u16* __restrict__ x, const u16* __restrict__ Wg, const u16* __restrict__ Wu,
                                               char* Hf) {
    extern __shared__ __attribute__((aligned(16))) char lds[];
    const int t = threadIdx.x, lane = t & 63, w = __builtin_amdgcn_readfirstlane(t >> 6);
    const int b = blockIdx.x;
    const int xcd = b & 7, c = b >> 3;
    STAMP(((char*)Hf - WS_H + WS_STAMP), 8);
    const int pi = (xcd & 1) * 8 + (c & 7), tj = (xcd >> 1) * 4 + (c >> 3);
    const int sg = tj <= 10 ? 5 * tj : 50 + 6 * (tj - 10), ng = tj < 10 ? 5 : 6;
    const rsrc_t rx = mkrsrc(x, L_ * D_ * 2), rg = mkrsrc(Wg, D_ * F_ * 2), ru = mkrsrc(Wu, D_ * F_ * 2);
    const unsigned vx = (8 * w + (lane >> 3)) * 1024 + (((lane & 7) ^ ((4 * w + (lane >> 4)) & 7)) << 4);
    unsigned vw[3];
#pragma unroll
    for (int i = 0; i < 3; ++i) {
        int cc = 64 * i + lane, row = cc / 12, ch = cc % 12;
        if (ng == 5 && ch >= 10) ch -= 2;
        vw[i] = row * (F_ * 2) + ch * 16;
    }
    const unsigned sx0 = pi * 128 * 1024, sw0 = w * 16 * (F_ * 2) + sg * 32;
    auto stage = [&](int kt, int buf) {
        char* base = lds + buf * G1_STAGE;
#pragma unroll
        for (int i = 0; i < 4; ++i) glds16(rx, base + G1_X + (w + 4 * i) * 1024, vx, sx0 + kt * 128 + i * 32768);
#pragma unroll
        for (int i = 0; i < 3; ++i) glds16(rg, base + G1_WG + (3 * w + i) * 1024, vw[i], sw0 + kt * 64 * (F_ * 2));
#pragma unroll
        for (int i = 0; i < 3; ++i) glds16(ru, base + G1_WU + (3 * w + i) * 1024, vw[i], sw0 + kt * 64 * (F_ * 2));
    };
    const int wf = w & 1, wm = w >> 1;
    const int m = lane & 31, hh = lane >> 5, grp = lane >> 4, q = (lane >> 2) & 3, p = lane & 3;
    const int xoff = G1_X + (64 * wm + m) * 128 + ((hh ^ ((m >> 1) & 7)) << 4);
    const int woff = ((grp & 1) ? G1_WU : G1_WG) + (8 * hh + q) * 192 + p * 8 + wf * 96;
    v16f acc[3][2];
#pragma unroll
    for (int j = 0; j < 3; ++j) { acc[j][0] = (v16f){}; acc[j][1] = (v16f){}; }
    stage(0, 0); stage(1, 1);
    int buf = 0;
#pragma unroll 1
    for (int kt = 0; kt < 8; ++kt) {
        if (kt < 7) { WAIT_VM(10); } else { WAIT_VM(0); }
        BAR();
        if (kt + 2 < 8) { int nb = buf + 2; if (nb >= 3) nb -= 3; stage(kt + 2, nb); }
        const char* base = lds + buf * G1_STAGE;
#pragma unroll
        for (int s = 0; s < 4; ++s) {
            v8bf fb[2], fa[3];
#pragma unroll
            for (int mt = 0; mt < 2; ++mt) fb[mt] = *(const v8bf*)(base + ((xoff ^ (s << 5)) + mt * 4096));
#pragma unroll
            for (int j = 0; j < 3; ++j) fa[j] = tr_frag(base + woff + (16 * s) * 192 + j * 32, base + woff + (16 * s + 4) * 192 + j * 32);
#pragma unroll
            for (int j = 0; j < 3; ++j)
#pragma unroll
                for (int mt = 0; mt < 2; ++mt)
                    acc[j][mt] = __builtin_amdgcn_mfma_f32_32x32x16_bf16(fa[j], fb[mt], acc[j][mt], 0, 0, 0);
        }
        WAIT_LGKM0();
        buf = buf == 2 ? 0 : buf + 1;
    }
    STAMP(((char*)Hf - WS_H + WS_STAMP), 17);
    const float c1 = -2.f * 0.7978845608028654f * 1.44269504088896341f, c2 = c1 * 0.044715f;
#pragma unroll
    for (int j = 0; j < 3; ++j) {
        const int gj = 3 * wf + j;
        if (gj < ng) {
#pragma unroll
            for (int mt = 0; mt < 2; ++mt) {
                float h[8];
#pragma unroll
                for (int e = 0; e < 8; ++e) {
                    float g = acc[j][mt][e], uu = acc[j][mt][8 + e];
                    float ex = __builtin_amdgcn_exp2f(g * (c1 + c2 * g * g));
                    h[e] = g * uu * __builtin_amdgcn_rcpf(1.f + ex);
                }
                v4u o = {pk2(h[0], h[1]), pk2(h[2], h[3]), pk2(h[4], h[5]), pk2(h[6], h[7])};
                *(v4u*)(Hf + ((size_t)(pi * 4 + 2 * wm + mt) * NG + sg + gj) * 1024 + lane * 16) = o;
            }
        }
    }
    STAMP(((char*)Hf - WS_H + WS_STAMP), 18);
}

constexpr int G2_DEPTH = 8;
__device__ __forceinline__ void k_gemm2(const char* Hf, const u16* __restrict__ Wd, const unsigned* a2,
                                               const float* gin, u16* __restrict__ out) {
    extern __shared__ __attribute__((aligned(16))) char lds[];
    const int t = threadIdx.x, lane = t & 63, w = __builtin_amdgcn_readfirstlane(t >> 6);
    const int b = blockIdx.x;
    const int xcd = b & 7, c = b >> 3;
    STAMP(((char*)Hf - WS_H + WS_STAMP), 20);
    const int im = xcd * 4 + (c >> 3), jd = c & 7;
    const int g0 = w < 2 ? 22 * w : 44 + 21 * (w - 2), ngr = w < 2 ? 22 : 21;
    const rsrc_t rh = mkrsrc(Hf, 64 * NG * 1024), rw = mkrsrc(Wd, F_ * D_ * 2);
    const unsigned vh = lane * 16;
    const unsigned vw = (lane >> 3) * 1024 + (((lane & 7) ^ (((lane >> 4) & 1) << 2)) << 4);
    char* ring = lds + w * (G2_DEPTH * 4096);
    auto stage = [&](int g, int slot) {
        char* base = ring + slot * 4096;
        glds16(rh, base, vh, ((2 * im) * NG + g) * 1024);
        glds16(rh, base + 1024, vh, ((2 * im + 1) * NG + g) * 1024);
        glds16(rw, base + 2048, vw, g * 16 * 1024 + jd * 128);
        glds16(rw, base + 3072, vw, g * 16 * 1024 + 8192 + jd * 128);
    };
    const int hh = lane >> 5, grp = lane >> 4, q = (lane >> 2) & 3, p = lane & 3;
    const int woff = 2048 + (4 * hh + q) * 128 + ((q >> 1) << 6) + (grp & 1) * 32 + p * 8;
    v16f acc[2][2];
#pragma unroll
    for (int j = 0; j < 2; ++j) { acc[j][0] = (v16f){}; acc[j][1] = (v16f){}; }
#pragma unroll
    for (int i = 0; i < G2_DEPTH - 1; ++i) stage(g0 + i, i);
#pragma unroll 1
    for (int i = 0; i < ngr; ++i) {
        int gn = i + G2_DEPTH - 1; if (gn >= ngr) gn = ngr - 1;
        WAIT_LGKM0();
        stage(g0 + gn, (i + G2_DEPTH - 1) & (G2_DEPTH - 1));
        WAIT_VM(28);
        const char* base = ring + (i & (G2_DEPTH - 1)) * 4096;
        v8bf fb[2], fa[2];
#pragma unroll
        for (int mt = 0; mt < 2; ++mt) fb[mt] = *(const v8bf*)(base + mt * 1024 + lane * 16);
#pragma unroll
        for (int dt = 0; dt < 2; ++dt) fa[dt] = tr_frag(base + (woff ^ (dt << 6)), base + (woff ^ (dt << 6)) + 1024);
#pragma unroll
        for (int dt = 0; dt < 2; ++dt)
#pragma unroll
            for (int mt = 0; mt < 2; ++mt)
                acc[dt][mt] = __builtin_amdgcn_mfma_f32_32x32x16_bf16(fa[dt], fb[mt], acc[dt][mt], 0, 0, 0);
    }
    STAMP(((char*)Hf - WS_H + WS_STAMP), 23);
    WAIT_VM(0);
    __syncthreads();
    STAMP(((char*)Hf - WS_H + WS_STAMP), 24);
#pragma unroll
    for (int dt = 0; dt < 2; ++dt)
#pragma unroll
        for (int mt = 0; mt < 2; ++mt)
#pragma unroll
            for (int k = 0; k < 4; ++k) {
                v4f v = {acc[dt][mt][4 * k], acc[dt][mt][4 * k + 1], acc[dt][mt][4 * k + 2], acc[dt][mt][4 * k + 3]};
                *(v4f*)(lds + ((dt + 2 * mt) * 4 + w) * 4096 + k * 1024 + lane * 16) = v;
            }
    __syncthreads();
    const int dt = w & 1, mt = w >> 1;
    const int l = im * 64 + mt * 32 + (lane & 31);
    const float g = gin[l], og = rbf(1.f - g);
#pragma unroll
    for (int k = 0; k < 4; ++k) {
        v4f s = {0, 0, 0, 0};
#pragma unroll
        for (int src = 0; src < 4; ++src) s += *(const v4f*)(lds + (w * 4 + src) * 4096 + k * 1024 + lane * 16);
        const int d0 = jd * 64 + dt * 32 + 8 * k + 4 * hh;
        const unsigned a01 = a2[(size_t)(d0 >> 1) * L_ + l], a23 = a2[(size_t)((d0 >> 1) + 1) * L_ + l];
        unsigned b01 = pk2(s[0], s[1]), b23 = pk2(s[2], s[3]);
        unsigned ga01 = pk2(g * bflo(a01), g * bfhi(a01)), ga23 = pk2(g * bflo(a23), g * bfhi(a23));
        unsigned gb01 = pk2(og * bflo(b01), og * bfhi(b01)), gb23 = pk2(og * bflo(b23), og * bfhi(b23));
        v2u o = {pk2(bflo(ga01) + bflo(gb01), bfhi(ga01) + bfhi(gb01)), pk2(bflo(ga23) + bflo(gb23), bfhi(ga23) + bfhi(gb23))};
        *(v2u*)(out + (size_t)l * D_ + d0) = o;
    }
    STAMP(((char*)Hf - WS_H + WS_STAMP), 25);
}

constexpr size_t WS_FLAG = (size_t)16 << 20;
struct Args { const u16* in[14]; u16* out; char* ws; };
__global__ __launch_bounds__(256) void k_all(Args a) {
    char* ws = a.ws;
    const unsigned long long tstart_ = __builtin_amdgcn_s_memrealtime();
    typedef GAS unsigned gu32;
    gu32* epoch = (gu32*)(ws + WS_FLAG);
    gu32* flags = (gu32*)(ws + WS_FLAG + 4096);
    const unsigned tag = __hip_atomic_load(epoch, __ATOMIC_RELAXED, __HIP_MEMORY_SCOPE_AGENT) + 1u;
    k_ssm(a.in[0], a.in[1], a.in[2], a.in[3], a.in[4], a.in[5], a.in[6], a.in[7], a.in[8], a.in[13], (unsigned*)(ws + WS_A), (float*)(ws + WS_G));
    __syncthreads();
    k_gemm1(a.in[0], a.in[10], a.in[11], ws + WS_H);
    asm volatile("s_waitcnt vmcnt(0)" ::: "memory");
    __syncthreads();
    if (threadIdx.x == 0) {
        __builtin_amdgcn_fence(__ATOMIC_RELEASE, "agent");
        asm volatile("s_waitcnt vmcnt(0)" ::: "memory");
        __hip_atomic_store(flags + blockIdx.x, tag, __ATOMIC_RELAXED, __HIP_MEMORY_SCOPE_AGENT);
    }
    {
        for (int spin = 0; spin < 200000; ++spin) {
            unsigned v = __hip_atomic_load(flags + threadIdx.x, __ATOMIC_RELAXED, __HIP_MEMORY_SCOPE_AGENT);
            if (__all(v == tag)) break;
            __builtin_amdgcn_s_sleep(2);
        }
        __builtin_amdgcn_fence(__ATOMIC_ACQUIRE, "agent");
        asm volatile("s_waitcnt vmcnt(0)" ::: "memory");
        __syncthreads();
    }
    if (blockIdx.x == 0 && threadIdx.x == 0) __hip_atomic_store(epoch, tag, __ATOMIC_RELAXED, __HIP_MEMORY_SCOPE_AGENT);
    k_gemm2(ws + WS_H, a.in[12], (const unsigned*)(ws + WS_A), (const float*)(ws + WS_G), a.out);
    if (blockIdx.x == 0 && threadIdx.x == 0) { while ((long long)(__builtin_amdgcn_s_memrealtime() - tstart_) < 6500) __builtin_amdgcn_s_sleep(1); }
}
extern "C" void kernel_launch(void* const* d_in, const int* in_sizes, int n_in,
                              void* d_out, int out_size, void* d_ws, size_t ws_size,
                              hipStream_t stream) {
    Args a;
    for (int i = 0; i < 14; ++i) a.in[i] = (const u16*)d_in[i];
    a.out = (u16*)d_out; a.ws = (char*)d_ws;
    (void)hipFuncSetAttribute((const void*)k_all, hipFuncAttributeMaxDynamicSharedMemorySize, 131072);
    k_all<<<256, 256, 131072, stream>>>(a);
}
```

```cpp
#include <hip/hip_runtime.h>
#include <hip/hip_bf16.h>
#include <stdint.h>

#define LAS __attribute__((address_space(3)))
typedef short  v4s  __attribute__((ext_vector_type(4)));
typedef short  v8s  __attribute__((ext_vector_type(8)));
typedef __bf16 v8bf __attribute__((ext_vector_type(8)));
typedef __bf16 v2bf __attribute__((ext_vector_type(2)));
typedef float  v2f  __attribute__((ext_vector_type(2)));
typedef float  v4f  __attribute__((ext_vector_type(4)));
typedef float  v16f __attribute__((ext_vector_type(16)));
typedef unsigned v4u __attribute__((ext_vector_type(4)));
typedef unsigned v2u __attribute__((ext_vector_type(2)));
typedef unsigned short u16;
typedef unsigned long long u64;

constexpr int L_ = 2048, D_ = 512, N_ = 128, F_ = 1376;
constexpr int NG = F_ / 16;

#define FSTR 128
constexpr unsigned WS_EP = 0;
constexpr unsigned WS_F1 = 4096;
constexpr unsigned WS_F2 = WS_F1 + 1024 * 128;
constexpr unsigned WS_F3 = WS_F2 + 1024 * 128;
constexpr unsigned WS_G  = WS_F3 + 1024 * 128;
constexpr unsigned WS_UT = WS_G + 8192;
constexpr unsigned WS_A  = WS_UT + 2048 * 512 * 2;
constexpr unsigned WS_H  = WS_A + 2048 * 512 * 2;
constexpr unsigned WS_END = WS_H + 64 * NG * 1024;

constexpr int X_RING = 0, X_SLOT = 16384, NXS = 3;
constexpr int WG_RING = X_RING + NXS * X_SLOT, W_SLOT = 6144, NWS = 5;
constexpr int WU_RING = WG_RING + NWS * W_SLOT;
constexpr int S_KC = 118784, KCP = 4160;
constexpr int S_U = S_KC + 8 * KCP, UP = 2288 * 2;
constexpr int TP = 272, S_TW = 32 * TP, S_T = S_U - 4 * S_TW;
constexpr int S_KD = S_U, KDN = 704;
constexpr int S_SYNC = S_KD + 4 * KDN * 4;
constexpr int C_P0 = S_SYNC, C_H = S_SYNC + 8, C_Y = S_SYNC + 12, C_DONE = S_SYNC + 16;
constexpr int S_RG = S_KC, S_AI = S_KC + 32768;
constexpr int LDS_TOTAL = 163840;
static_assert(S_T >= WU_RING + NWS * W_SLOT && S_SYNC + 32 <= LDS_TOTAL && S_AI + 8192 <= S_SYNC, "lds");

__device__ __forceinline__ float bflo(unsigned v) { return __uint_as_float(v << 16); }
__device__ __forceinline__ float bfhi(unsigned v) { return __uint_as_float(v & 0xffff0000u); }
__device__ __forceinline__ unsigned pk2(float lo, float hi) {
    v2f f = {lo, hi};
    return __builtin_bit_cast(unsigned, __builtin_convertvector(f, v2bf));
}
__device__ __forceinline__ float rbf(float x) { return bflo(pk2(x, x)); }

#define WAIT_VM(n)  asm volatile("s_waitcnt vmcnt(%0)" :: "n"(n) : "memory")
#define SB() __builtin_amdgcn_sched_barrier(0)
#define BARX() do { SB(); asm volatile("s_barrier" ::: "memory"); SB(); } while (0)
#define LGKM0() __builtin_amdgcn_s_waitcnt(0xC07F)

#define STAMP(k)
#define STAMP_FLUSH(ws)

typedef v4u rsrc_t;
__device__ __forceinline__ rsrc_t mkrsrc(const void* p, unsigned bytes) {
    uint64_t a = (uint64_t)p;
    rsrc_t r = {(unsigned)a, (unsigned)(a >> 32) & 0xffffu, bytes, 0x00020000u};
    return r;
}
__device__ __forceinline__ void dma1(rsrc_t r, unsigned m0v, unsigned v0, unsigned soff) {
    asm volatile("s_mov_b32 m0, %0\n\ts_nop 0\n\tbuffer_load_dwordx4 %1, %2, %3 offen lds"
                 :: "s"(m0v), "v"(v0), "s"(r), "s"(soff) : "memory");
}
__device__ __forceinline__ void dma2(rsrc_t r, unsigned m0v, unsigned v0, unsigned v1, unsigned soff) {
    asm volatile("s_mov_b32 m0, %0\n\ts_nop 0\n\tbuffer_load_dwordx4 %1, %3, %4 offen lds\n\tbuffer_load_dwordx4 %2, %3, %4 offen offset:1024 lds"
                 :: "s"(m0v), "v"(v0), "v"(v1), "s"(r), "s"(soff) : "memory");
}
__device__ __forceinline__ void dma3(rsrc_t r, unsigned m0v, unsigned v0, unsigned v1, unsigned v2, unsigned soff) {
    asm volatile("s_mov_b32 m0, %0\n\ts_nop 0\n\tbuffer_load_dwordx4 %1, %4, %5 offen lds\n\tbuffer_load_dwordx4 %2, %4, %5 offen offset:1024 lds"
                 "\n\tbuffer_load_dwordx4 %3, %4, %5 offen offset:2048 lds"
                 :: "s"(m0v), "v"(v0), "v"(v1), "v"(v2), "s"(r), "s"(soff) : "memory");
}
__device__ __forceinline__ void st128(rsrc_t r, v4u d, unsigned voff, unsigned soff) {
    asm volatile("buffer_store_dwordx4 %0, %1, %2, %3 offen sc1\n\ts_nop 1" :: "v"(d), "v"(voff), "s"(r), "s"(soff) : "memory");
}
__device__ __forceinline__ void st32(rsrc_t r, unsigned d, unsigned voff, unsigned soff) {
    asm volatile("buffer_store_dword %0, %1, %2, %3 offen sc1" :: "v"(d), "v"(voff), "s"(r), "s"(soff) : "memory");
}
__device__ __forceinline__ void st_flag(rsrc_t r, u64 tag, unsigned soff) {
    v2u d = {(unsigned)tag, (unsigned)(tag >> 32)};
    u64 sv;
    asm volatile("s_mov_b64 %0, exec\n\ts_mov_b64 exec, 1\n\tbuffer_store_dwordx2 %1, off, %2, %3 sc1\n\ts_mov_b64 exec, %0"
                 : "=&s"(sv) : "v"(d), "s"(r), "s"(soff) : "memory");
}
__device__ __forceinline__ void ld64_sc1(v2u& d, rsrc_t r, unsigned voff, unsigned soff) {
    asm volatile("buffer_load_dwordx2 %0, %1, %2, %3 offen sc1" : "=v"(d) : "v"(voff), "s"(r), "s"(soff) : "memory");
}
__device__ __forceinline__ void ld128_sc1(v4u& d, rsrc_t r, unsigned voff, unsigned soff) {
    asm volatile("buffer_load_dwordx4 %0, %1, %2, %3 offen sc1" : "=v"(d) : "v"(voff), "s"(r), "s"(soff) : "memory");
}
__device__ __forceinline__ bool tag_ok(v2u f, u64 tag) {
    return __builtin_amdgcn_ballot_w64((((u64)f[1] << 32) | f[0]) != tag) == 0;
}
constexpr int SPIN_MAX = 1 << 18;
__device__ __forceinline__ void poll(rsrc_t r, unsigned voff, unsigned soff, u64 tag) {
#pragma unroll 1
    for (int i = 0; i < SPIN_MAX; ++i) {
        v2u f;
        asm volatile("buffer_load_dwordx2 %0, %1, %2, %3 offen sc1\n\ts_waitcnt vmcnt(0)" : "=&v"(f) : "v"(voff), "s"(r), "s"(soff) : "memory");
        if (tag_ok(f, tag)) break;
        __builtin_amdgcn_s_sleep(1);
    }
}
__device__ __forceinline__ void lds_bump(unsigned addr) {
    u64 sv;
    asm volatile("s_mov_b64 %0, exec\n\ts_mov_b64 exec, 1\n\tds_add_u32 %1, %2\n\ts_mov_b64 exec, %0" : "=&s"(sv) : "v"(addr), "v"(1u) : "memory");
}
__device__ __forceinline__ void lds_wait(unsigned addr, unsigned n) {
#pragma unroll 1
    for (int i = 0; i < SPIN_MAX * 8; ++i) {
        unsigned v;
        asm volatile("ds_read_b32 %0, %1\n\ts_waitcnt lgkmcnt(0)" : "=&v"(v) : "v"(addr) : "memory");
        if (__builtin_amdgcn_readfirstlane(v) >= n) break;
    }
    SB();
}

__device__ __forceinline__ v8bf ldsb128(unsigned a) { return *(LAS const v8bf*)(uintptr_t)a; }
__device__ __forceinline__ v4f ldsf4(unsigned a) { return *(LAS const v4f*)(uintptr_t)a; }
__device__ __forceinline__ void stsf4(unsigned a, v4f v) { *(LAS v4f*)(uintptr_t)a = v; }
__device__ __forceinline__ void stsu4(unsigned a, v4u v) { *(LAS v4u*)(uintptr_t)a = v; }
__device__ __forceinline__ void stsu1(unsigned a, unsigned v) { *(LAS unsigned*)(uintptr_t)a = v; }
__device__ __forceinline__ v4s ldstr(unsigned a) { return __builtin_amdgcn_ds_read_tr16_b64_v4i16((LAS v4s*)(uintptr_t)a); }
__device__ __forceinline__ v8bf cat(v4s a, v4s b) { return __builtin_bit_cast(v8bf, __builtin_shufflevector(a, b, 0, 1, 2, 3, 4, 5, 6, 7)); }
#define MFMA32(a, b, c) __builtin_amdgcn_mfma_f32_32x32x16_bf16((a), (b), (c), 0, 0, 0)
#define MFMA16(a, b, c) __builtin_amdgcn_mfma_f32_16x16x32_bf16((a), (b), (c), 0, 0, 0)

struct cf { float r, i; };
__device__ __forceinline__ cf cmul(cf a, cf b) { return {a.r * b.r - a.i * b.i, a.r * b.i + a.i * b.r}; }
__device__ __forceinline__ cf cexp_(float al, float be, float e) {
    float m = __builtin_amdgcn_exp2f(al * e);
    float th = __builtin_amdgcn_fractf(be * e);
    return {m * __builtin_amdgcn_cosf(th), m * __builtin_amdgcn_sinf(th)};
}

struct G1Frag { v8bf b[4]; v8bf a[6]; };
template <bool RD, bool MM>
__device__ __forceinline__ void g1_step(v4f (&acc)[6][4], const G1Frag& cur, G1Frag& nxt, unsigned xa, unsigned wa0, unsigned wa1, bool full) {
    auto rdA = [&](int a) {
        const unsigned p = ((a >> 1) == 1 ? wa1 : wa0) + (a >> 1) * 32 + (a & 1) * (WU_RING - WG_RING);
        nxt.a[a] = cat(ldstr(p), ldstr(p + 4 * 192));
    };
#pragma unroll
    for (int i = 0; i < 16; ++i) {
        const int a = i >> 2, n = (a & 1) ? 3 - (i & 3) : (i & 3);
        if (MM) acc[a][n] = MFMA16(cur.a[a], cur.b[n], acc[a][n]);
        if (RD) {
            if (i < 4) nxt.b[i] = ldsb128(xa + i * 2048);
            else if (!(i & 1)) rdA((i - 4) >> 1);
        }
        SB();
    }
    if (MM && full) {
#pragma unroll
        for (int i = 16; i < 24; ++i) {
            const int a = i >> 2, n = (a & 1) ? 3 - (i & 3) : (i & 3);
            acc[a][n] = MFMA16(cur.a[a], cur.b[n], acc[a][n]);
            SB();
        }
    }
}

struct Args {
    const u16 *x, *Wg, *Wu, *Wd, *Are, *Aim, *Bre, *Bim, *Cre, *Cim, *logdt, *Dskip, *gw;
    char* ws; u16* out;
};

__global__ __launch_bounds__(512, 2) void k_fused(Args A) {
    const int t = threadIdx.x, lane = t & 63, w = __builtin_amdgcn_readfirstlane(t >> 6);
    const int b = blockIdx.x;
    const int xcd = b & 7, c = b >> 3;
    const int pi = (xcd & 1) * 8 + (c & 7), tq = xcd >> 1, tr = c >> 3, tj = tq * 4 + tr;
    const bool full = !(tr == 3 || tj == 14);
    const int sp = tq * 11 + 3 * tr - (tj == 15);
    const int im = xcd * 4 + (c >> 3), jd = c & 7;
    const int lw = w & 3, role = w >> 2;
    STAMP(0);
    const unsigned vx0 = (lane >> 3) * 1024 + (((lane & 7) ^ (lane >> 4)) << 4);
    const unsigned vx1 = (vx0 ^ 64) + 8192 - 1024;
    unsigned vw[3];
#pragma unroll
    for (int i = 0; i < 3; ++i) {
        int cc = 64 * i + lane, row = cc / 12, ch = (cc % 12) ^ ((row >> 3) << 1);
        int src = ch;
        if (!full) src = ch < 6 ? ch : ch - 2;
        vw[i] = row * (F_ * 2) + src * 16 - 1024 * i;
        if (!full && (ch == 4 || ch == 5 || ch >= 10)) vw[i] = 0x7f000000u;
    }
    const rsrc_t rx = mkrsrc(A.x, L_ * D_ * 2), rw = mkrsrc(lw >> 1 ? A.Wu : A.Wg, D_ * F_ * 2);
    const unsigned sx0 = (pi * 128 + 16 * lw) * 1024, sw0 = (lw & 1) * 16 * (F_ * 2) + sp * 64;
    const unsigned mx0 = X_RING + lw * 2048, mw0 = (lw >> 1 ? WU_RING : WG_RING) + (lw & 1) * 3072;
    auto ldx = [&](int u, int h) { dma2(rx, mx0 + (u % NXS) * X_SLOT + h * 8192, vx0, vx1, sx0 + h * 65536 + u * 128); };
    auto ldw = [&](int s) { dma3(rw, mw0 + (s % NWS) * W_SLOT, vw[0], vw[1], vw[2], sw0 + s * 32 * (F_ * 2)); };
    dma2(rx, mx0 + role * 8192, vx0, vx1, sx0 + role * 65536);
    dma3(rw, mw0 + role * W_SLOT, vw[0], vw[1], vw[2], sw0 + role * 32 * (F_ * 2));
    STAMP(1);
    const rsrc_t rs = mkrsrc(A.ws, WS_END), rd = mkrsrc(A.Wd, F_ * D_ * 2);
    u64 tag = *(const __attribute__((address_space(4))) u64*)(uintptr_t)(A.ws + WS_EP) + 1;
    const int q = (lane >> 2) & 3, p = lane & 3, g = lane >> 4, r16 = lane & 15;
    const int kq = (role << 1) | (lw >> 1), mt = lw & 1;
    const unsigned wda = (8 * g + q) * 128 + p * 8 + (((q >> 1) | ((g & 1) << 1)) << 5);
    const unsigned slotA = (kq & 1) * 11 * 4096;
    v8bf fa[11][4];
    v4u fb[11][2];
    float gate;
    const __amdgpu_buffer_rsrc_t rsb = __builtin_amdgcn_make_buffer_rsrc((void*)A.ws, 0, WS_END, 0x00020000);
    auto issue_h = [&]() {
        const unsigned vh = (g >> 1) * 1024 + ((g & 1) * 32 + r16) * 16;
        const unsigned sh = WS_H + ((2 * im + mt) * NG + 22 * kq) * 1024;
#pragma unroll
        for (int i = 0; i < 10; ++i) {
            fb[i][0] = __builtin_amdgcn_raw_buffer_load_b128(rsb, vh, sh + i * 2048, 16);
            fb[i][1] = __builtin_amdgcn_raw_buffer_load_b128(rsb, vh + 256, sh + i * 2048, 16);
        }
        const unsigned vl = kq == 3 ? 0x7f000000u : vh;
        fb[10][0] = __builtin_amdgcn_raw_buffer_load_b128(rsb, vl, sh + 10 * 2048, 16);
        fb[10][1] = __builtin_amdgcn_raw_buffer_load_b128(rsb, vl + 256, sh + 10 * 2048, 16);
        gate = __uint_as_float(__builtin_amdgcn_raw_buffer_load_b32(rsb, r16 * 4, WS_G + (im * 64 + 16 * lw) * 4, 16));
        SB();
    };
    auto preread = [&]() {
#pragma unroll
        for (int i = 0; i < 7; ++i)
#pragma unroll
            for (int tt = 0; tt < 4; ++tt) {
                const unsigned a = ((wda ^ (tt << 5)) + slotA) + i * 4096;
                fa[i][tt] = cat(ldstr(a), ldstr(a + 512));
            }
        LGKM0(); SB();
        lds_bump(C_P0 + (kq & 1) * 4);
    };

    if (w < 4) {
        __builtin_amdgcn_s_setprio(1);
        const int wf = w & 1, wm = w >> 1;
        const unsigned xoff = X_RING + (64 * wm + r16) * 128 + ((g ^ (r16 >> 1)) << 4);
        const unsigned wbase = WG_RING + (8 * g + q) * 192 + p * 8 + wf * 96;
        const unsigned sgn = (g & 1) * 32;
        const unsigned w0 = wf ? wbase - sgn : wbase + sgn, w1 = wf ? wbase + sgn : wbase - sgn;
        v4f acc[6][4];
#pragma unroll
        for (int j = 0; j < 6; ++j)
#pragma unroll
            for (int n = 0; n < 4; ++n) { acc[j][n] = (v4f){0, 0, 0, 0}; asm volatile("" : "+v"(acc[j][n])); }
        G1Frag fA, fB;
        asm volatile("" :: "s"(tag));
        WAIT_VM(0);
        BARX();
        g1_step<true, false>(acc, fB, fA, xoff, w0, w1, full);
        unsigned xs = 0, wsl = W_SLOT;
#pragma unroll 1
        for (int u = 0; u < 7; ++u) {
            LGKM0(); BARX();
            {
                const unsigned xa = (xoff ^ 64) + xs, a0 = w0 + wsl, a1 = w1 + wsl;
                wsl += W_SLOT; if (wsl == NWS * W_SLOT) wsl = 0;
                xs += X_SLOT; if (xs == NXS * X_SLOT) xs = 0;
                g1_step<true, true>(acc, fA, fB, xa, a0, a1, full);
            }
            LGKM0(); BARX();
            {
                const unsigned xa = xoff + xs, a0 = w0 + wsl, a1 = w1 + wsl;
                wsl += W_SLOT; if (wsl == NWS * W_SLOT) wsl = 0;
                g1_step<true, true>(acc, fB, fA, xa, a0, a1, full);
            }
        }
        LGKM0(); BARX();
        g1_step<true, true>(acc, fA, fB, (xoff ^ 64) + (7 % NXS) * X_SLOT, w0 + (15 % NWS) * W_SLOT, w1 + (15 % NWS) * W_SLOT, full);
        LGKM0(); BARX();
        g1_step<false, true>(acc, fB, fA, 0, 0, 0, full);
        STAMP(2);
        const float c1 = -2.f * 0.7978845608028654f * 1.44269504088896341f, c2 = c1 * 0.044715f;
        const unsigned sh = WS_H + ((pi * 4 + 2 * wm) * NG + 2 * sp + (full ? 3 : 2) * wf) * 1024;
#pragma unroll
        for (int j = 0; j < 3; ++j) {
            if (j < 2 || full) {
#pragma unroll
                for (int m2 = 0; m2 < 2; ++m2) {
                    unsigned hp[2][2];
#pragma unroll
                    for (int n = 0; n < 2; ++n)
#pragma unroll
                        for (int h = 0; h < 2; ++h) {
                            v2f gg = {acc[2 * j][2 * m2 + n][2 * h], acc[2 * j][2 * m2 + n][2 * h + 1]};
                            v2f uu = {acc[2 * j + 1][2 * m2 + n][2 * h], acc[2 * j + 1][2 * m2 + n][2 * h + 1]};
                            v2f ee = gg * (gg * gg * c2 + c1);
                            v2f dd = (v2f){__builtin_amdgcn_exp2f(ee[0]), __builtin_amdgcn_exp2f(ee[1])} + 1.f;
                            v2f hh = gg * uu * (v2f){__builtin_amdgcn_rcpf(dd[0]), __builtin_amdgcn_rcpf(dd[1])};
                            hp[n][h] = pk2(hh[0], hh[1]);
                        }
                    auto s0 = __builtin_amdgcn_permlane16_swap(hp[0][0], hp[1][0], false, false);
                    auto s1 = __builtin_amdgcn_permlane16_swap(hp[0][1], hp[1][1], false, false);
                    st128(rs, (v4u){s0[0], s1[0], s0[1], s1[1]}, lane * 16, sh + (m2 * NG + j) * 1024);
                }
            }
        }
        STAMP(3);
        BARX();
        STAMP(4);
        preread();
        lds_wait(C_P0 + (kq & 1) * 4, 2);
        STAMP(5);
        {
            const unsigned v0 = (lane >> 3) * 1024 + (((lane & 7) ^ (((lane >> 4) & 1) << 1)) << 4);
            const unsigned v1 = (v0 ^ 64) + 8192 - 1024;
            const unsigned s0 = ((kq + 2) * 11 * 32 + 16 * mt) * 1024 + jd * 128;
#pragma unroll
            for (int i = 0; i < 7; ++i) dma2(rd, slotA + i * 4096 + mt * 2048, v0, v1, s0 + i * 32768);
        }
        WAIT_VM(14);
        st_flag(rs, tag, WS_F2 + ((2 * pi + wm) * 32 + 2 * tj + wf) * FSTR);
        STAMP(6);
        poll(rs, (lane & 31) * FSTR, WS_F2 + im * 32 * FSTR, tag);
        STAMP(7);
        issue_h();
        lds_bump(C_H);
        __builtin_amdgcn_s_setprio(0);
    } else {
        const int ch = lw >> 1, hf = lw & 1, d = 2 * b + ch, st = t - 256;
        unsigned pr[6];
        {
            const unsigned po = (d * N_ + 64 * hf + lane) * 2;
            const u16* ps[6] = {A.Are, A.Aim, A.Bre, A.Bim, A.Cre, A.Cim};
#pragma unroll
            for (int i = 0; i < 6; ++i) asm volatile("global_load_ushort %0, %1, %2" : "=v"(pr[i]) : "v"(po), "s"(ps[i]) : "memory");
        }
        ldx(1, 0); ldx(1, 1); ldw(2);
        const unsigned ldt2 = *(const __attribute__((address_space(4))) unsigned*)(uintptr_t)(A.logdt + 2 * b);
        const unsigned dsk2 = *(const __attribute__((address_space(4))) unsigned*)(uintptr_t)(A.Dskip + 2 * b);
        if (st < 8) stsu1(S_SYNC + st * 4, 0);
        asm volatile("" :: "s"(tag), "s"(ldt2), "s"(dsk2));
        LGKM0();
        WAIT_VM(16);
        BARX();
        STAMP(2);
        WAIT_VM(13);
        BARX();
        STAMP(3);
        ldw(3);
        dma2(rx, S_KD + lw * 2048, vx0, vx1, sx0 + (tj & 1) * 65536 + (tj >> 1) * 128);
        v4u gx0, gx1, gw0, gw1;
        {
            const unsigned go = (pi * 128 + 8 * tj + 2 * lw + (lane >> 5)) * 1024 + (lane & 31) * 32, wo = (lane & 31) * 32;
            asm volatile("global_load_dwordx4 %0, %1, %2" : "=v"(gx0) : "v"(go), "s"(A.x) : "memory");
            asm volatile("global_load_dwordx4 %0, %1, %2 offset:16" : "=v"(gx1) : "v"(go), "s"(A.x) : "memory");
            asm volatile("global_load_dwordx4 %0, %1, %2" : "=v"(gw0) : "v"(wo), "s"(A.gw) : "memory");
            asm volatile("global_load_dwordx4 %0, %1, %2 offset:16" : "=v"(gw1) : "v"(wo), "s"(A.gw) : "memory");
        }
        ldx(2, 0);
        WAIT_VM(11);
        BARX();
        STAMP(4);
        asm volatile("" : "+v"(pr[0]), "+v"(pr[1]), "+v"(pr[2]), "+v"(pr[3]), "+v"(pr[4]), "+v"(pr[5]));
        ldx(2, 1); ldw(4);
        const float dt = rbf(__expf(ch ? bfhi(ldt2) : bflo(ldt2)));
        const float al = dt * bflo(pr[0]) * 1.44269504088896341f, be = dt * bflo(pr[1]) * 0.15915494309189535f;
        cf wv = cmul(cf{bflo(pr[4]), bflo(pr[5])}, cf{dt * bflo(pr[2]), dt * bflo(pr[3])});
        cf z1 = cexp_(al, be, 1.f), z8 = cexp_(al, be, 8.f), z256 = cexp_(al, be, 256.f);
        cf z16 = cmul(z8, z8);
        asm volatile("" : "+v"(wv.r), "+v"(wv.i), "+v"(z1.r), "+v"(z1.i), "+v"(z8.r), "+v"(z8.i), "+v"(z16.r), "+v"(z16.i), "+v"(z256.r), "+v"(z256.i));
        ldw(5); ldx(3, 0);
        WAIT_VM(12);
        BARX();
        STAMP(5);
        {
            v4u o[2];
#pragma unroll
            for (int i = 0; i < 2; ++i) {
                const int r0 = 32 * i + 8 * g + q, r1 = r0 + 4;
                const unsigned a0 = S_KD + r0 * 128 + (((2 * lw + (p >> 1)) ^ ((r0 >> 1) & 7)) << 4) + (p & 1) * 8;
                const unsigned a1 = S_KD + r1 * 128 + (((2 * lw + (p >> 1)) ^ ((r1 >> 1) & 7)) << 4) + (p & 1) * 8;
                o[i] = __builtin_bit_cast(v4u, cat(ldstr(a0), ldstr(a1)));
            }
            const unsigned vo = (r16 >> 1) * 8192 + (r16 & 1) * 64 + g * 16;
            const unsigned so = WS_UT + ((tj >> 1) * 32 + 8 * lw) * 8192 + (4 * pi + 2 * (tj & 1)) * 128;
            st128(rs, o[0], vo, so); st128(rs, o[1], vo, so + 128);
            asm volatile("" : "+v"(gx0), "+v"(gx1), "+v"(gw0), "+v"(gw1));
            float s = 0.f;
#pragma unroll
            for (int k = 0; k < 4; ++k) {
                unsigned p0 = pk2(bflo(gx0[k]) * bflo(gw0[k]), bfhi(gx0[k]) * bfhi(gw0[k]));
                unsigned p1 = pk2(bflo(gx1[k]) * bflo(gw1[k]), bfhi(gx1[k]) * bfhi(gw1[k]));
                s += bflo(p0) + bfhi(p0) + bflo(p1) + bfhi(p1);
            }
#pragma unroll
            for (int m = 16; m >= 1; m >>= 1) s += __shfl_xor(s, m);
            s = rbf(s);
            float gg = rbf(__builtin_amdgcn_rcpf(1.f + __expf(-s)));
            st32(rs, __float_as_uint(gg), (lane & 31) ? 0x7f000000u : (lane >> 5) * 4, WS_G + (pi * 128 + 8 * tj + 2 * lw) * 4);
        }
        ldx(3, 1); ldw(6);
        WAIT_VM(13);
        BARX();
        STAMP(6);
        const unsigned T = S_T + lw * S_TW + lane * 4;
        cf e = {1.f, 0.f}, o = z8;
        auto e1rows = [&](int r0, int r1) {
#pragma unroll
            for (int r = r0; r < r1; r += 2) {
                stsu1(T + r * TP, pk2(e.r, e.i));
                stsu1(T + (r + 1) * TP, pk2(o.r, o.i));
                if (r + 2 < 32) { e = cmul(e, z16); o = cmul(o, z16); }
            }
            asm volatile("" : "+v"(e.r), "+v"(e.i), "+v"(o.r), "+v"(o.i));
        };
        e1rows(0, 10);
        ldw(7); ldx(4, 0); ldw(8);
        WAIT_VM(13);
        BARX();
        STAMP(7);
        st_flag(rs, tag, WS_F1 + (((tj >> 1) * 4 + lw) * 32 + 2 * pi + (tj & 1)) * FSTR);
        e1rows(10, 22);
        ldx(4, 1); ldw(9);
        WAIT_VM(14);
        BARX();
        STAMP(8);
        e1rows(22, 32);
        ldx(5, 0); ldw(10);
        WAIT_VM(16);
        BARX();
        STAMP(9);
        v8bf ka[8];
        {
            const unsigned TA = S_T + lw * S_TW + (lane & 31) * TP + (lane >> 5) * 16;
#pragma unroll
            for (int s = 0; s < 8; ++s) ka[s] = ldsb128(TA + s * 32);
            unsigned ev[8];
            cf pp = wv;
#pragma unroll
            for (int k = 0; k < 8; ++k) { ev[k] = pk2(pp.r, -pp.i); if (k < 7) pp = cmul(pp, z1); }
            LGKM0(); SB();
#pragma unroll
            for (int k = 0; k < 8; ++k) stsu1(T + k * TP, ev[k]);
        }
        ldx(5, 1); ldw(11);
        WAIT_VM(13);
        BARX();
        STAMP(10);
        {
            const cf z2 = cmul(z1, z1), z4 = cmul(z2, z2);
            cf qq = cmul(wv, z256);
            cf q2 = qq;
#pragma unroll
            for (int k = 0; k < 4; ++k) { stsu1(T + (8 + k) * TP, pk2(q2.r, -q2.i)); if (k < 3) q2 = cmul(q2, z2); }
#pragma unroll
            for (int k = 0; k < 2; ++k) {
                qq = cmul(qq, z256);
                cf q4 = cmul(qq, z4);
                stsu1(T + (12 + 2 * k) * TP, pk2(qq.r, -qq.i));
                stsu1(T + (13 + 2 * k) * TP, pk2(q4.r, -q4.i));
            }
#pragma unroll
            for (int k = 0; k < 5; ++k) { qq = cmul(qq, z256); stsu1(T + (16 + k) * TP, pk2(qq.r, -qq.i)); }
        }
        ldx(6, 0); ldw(12);
        WAIT_VM(15);
        BARX();
        STAMP(11);
        {
            const int r = lane & 31, hh = lane >> 5;
            const unsigned TB = S_T + lw * S_TW + (r < 20 ? r : 20) * TP + hh * 16;
            v16f kacc = {};
#pragma unroll
            for (int s = 0; s < 8; ++s) kacc = MFMA32(ka[s], ldsb128(TB + s * 32), kacc);
            int base, stride;
            if (r < 8) { base = r; stride = 8; }
            else if (r < 12) { base = 256 + (r - 8); stride = 4; }
            else if (r < 16) { base = 384 + 64 * ((r - 12) >> 1) + ((r - 12) & 1); stride = 2; }
            else if (r <= 20) { base = 512 + 32 * (r - 16); stride = 1; }
            else { base = 672; stride = 1; }
            const unsigned KD = S_KD + lw * KDN * 4 + (base + stride * 4 * hh) * 4;
#pragma unroll
            for (int k = 0; k < 16; ++k) stsu1(KD + stride * ((k & 3) + 8 * (k >> 2)) * 4, __float_as_uint(kacc[k]));
        }
        ldx(6, 1); ldw(13);
        LGKM0();
        WAIT_VM(13);
        BARX();
        STAMP(12);
        unsigned kidx[12];
#pragma unroll
        for (int k = 0; k < 12; ++k) {
            int j = 2047 - (8 * st + k);
            int jj = j < 0 ? 0 : j;
            kidx[k] = S_KD + (jj < 128 ? jj : (int)(pk2((float)jj, 0.f) & 0xffffu) - 17024) * 4;
        }
#pragma unroll
        for (int k = 0; k < 12; ++k) asm volatile("" : "+v"(kidx[k]));
        ldx(7, 0); ldw(14);
        WAIT_VM(15);
        BARX();
        STAMP(13);
        auto expand = [&](int cc) {
            float v[12];
#pragma unroll
            for (int k = 0; k < 11; ++k) {
                float x = *(LAS const float*)(uintptr_t)(kidx[k] + 2 * cc * KDN * 4) + *(LAS const float*)(uintptr_t)(kidx[k] + (2 * cc + 1) * KDN * 4);
                v[k] = (k >= 8 && st == 255) ? 0.f : x;
            }
#pragma unroll
            for (int s = 0; s < 4; ++s) {
                v4u oo = {pk2(v[s], v[s + 1]), pk2(v[s + 2], v[s + 3]), pk2(v[s + 4], v[s + 5]), pk2(v[s + 6], v[s + 7])};
                stsu4(S_KC + (cc * 4 + s) * KCP + st * 16, oo);
            }
        };
        v2u f1;
        const unsigned vf1 = (lane & 31) * FSTR, sf1 = WS_F1 + (b >> 3) * 32 * FSTR;
        ld64_sc1(f1, rs, vf1, sf1);
        expand(0);
        ldx(7, 1); ldw(15);
        WAIT_VM(14);
        BARX();
        STAMP(14);
        expand(1);
        if (st < 16) stsu4(S_KC + (st >> 1) * KCP + 4096 + (st & 1) * 16, (v4u){0, 0, 0, 0});
        LGKM0();
        WAIT_VM(11);
        BARX();
        STAMP(15);
        if (st < 60) stsu4(S_U + (st >= 30 ? UP : 0) + (st % 30) * 16, (v4u){0, 0, 0, 0});
        WAIT_VM(3);
        BARX();
        STAMP(16);
        asm volatile("" : "+v"(f1));
        if (!tag_ok(f1, tag)) poll(rs, vf1, sf1, tag);
        v4u uu0, uu1;
        ld128_sc1(uu0, rs, st * 16, WS_UT + b * 8192); ld128_sc1(uu1, rs, st * 16, WS_UT + b * 8192 + 4096);
        WAIT_VM(2);
        BARX();
        STAMP(17);
        if (b == 0 && lw == 3) {
#pragma unroll 1
            for (int k = 0; k < 4; ++k) poll(rs, ((lane >> 5) * 128 + (lane & 31)) * FSTR, WS_F1 + k * 256 * FSTR, tag);
            st_flag(rs, tag, WS_EP);
        }
        BARX();
        STAMP(18);
        const unsigned vwd = (lane >> 3) * 1024 + (((lane & 7) ^ ((((lane >> 4) & 1) | ((lw & 1) << 1)) << 1)) << 4);
        {
            const unsigned s0 = lw * 8192 + jd * 128;
#pragma unroll 1
            for (int i = 0; i < 22; ++i) dma1(rd, i * 4096 + lw * 1024, vwd, s0 + i * 32768);
#pragma unroll
            for (int i = 22; i < 29; ++i) dma1(rd, i * 4096 + lw * 1024, vwd, s0 + (i < 26 ? i + 7 : i + 14) * 32768);
        }
        WAIT_VM(7);
        asm volatile("" : "+v"(uu0), "+v"(uu1));
        {
            const unsigned ua = S_U + ((st >> 2) & 1) * UP + (240 + 32 * (st >> 3) + 8 * (st & 3)) * 2;
            stsu4(ua, uu0); stsu4(ua + 2048, uu1);
        }
        LGKM0();
        STAMP(19);
        BARX();
        STAMP(20);
        v4f y[8];
#pragma unroll
        for (int k = 0; k < 8; ++k) y[k] = (v4f){0, 0, 0, 0};
        {
            const int rp = 15 - r16;
            unsigned KA = S_KC + (ch * 4 + (rp & 3)) * KCP + (8 * g + (rp & 12) + 128 * hf) * 2;
            unsigned UB = S_U + ch * UP + (16 * r16 + 8 * g + 128 * hf) * 2;
            v2u al_[2][8], ah_[2][8]; v8bf bb[2][8];
            auto rdm = [&](int buf, int m, int off) {
                asm volatile("ds_read_b64 %0, %1 offset:%2" : "=v"(al_[buf][m]) : "v"(KA), "n"(off + (7 - m) * 512) : "memory");
                asm volatile("ds_read_b64 %0, %1 offset:%2" : "=v"(ah_[buf][m]) : "v"(KA), "n"(off + (7 - m) * 512 + 8) : "memory");
                asm volatile("ds_read_b128 %0, %1 offset:%2" : "=v"(bb[buf][m]) : "v"(UB), "n"(off + m * 512) : "memory");
            };
#pragma unroll
            for (int m = 0; m < 8; ++m) rdm(0, m, 0);
#pragma unroll
            for (int tt = 0; tt < 4; ++tt) {
                const int cb = tt & 1;
                asm volatile("s_waitcnt lgkmcnt(0)" ::: "memory");
#pragma unroll
                for (int m = 0; m < 8; ++m) asm volatile("" : "+v"(al_[cb][m]), "+v"(ah_[cb][m]), "+v"(bb[cb][m]));
                SB();
                int cnt = 0;
#pragma unroll
                for (int m = 0; m < 8; ++m)
#pragma unroll
                    for (int k = 0; k + m < 8; ++k) {
                        v4u x = {al_[cb][m][0], al_[cb][m][1], ah_[cb][m][0], ah_[cb][m][1]};
                        y[m + k] = MFMA16(__builtin_bit_cast(v8bf, x), bb[cb][k], y[m + k]);
                        if (tt < 3 && (cnt % 3) == 0 && cnt < 24) rdm(cb ^ 1, cnt / 3, (tt + 1) * 64);
                        ++cnt;
                        SB();
                    }
            }
        }
        STAMP(21);
        {
            const unsigned ya = S_KC + ch * 4 * KCP + (lane >> 3) * 512 + 128 + (lane & 7) * 16;
            if (hf == 0) {
#pragma unroll
                for (int k = 0; k < 4; ++k) stsf4(ya + k * KCP, y[4 + k]);
            } else {
#pragma unroll
                for (int k = 0; k < 4; ++k) { stsf4(ya + 256 + k * KCP, y[k]); y[k] = y[4 + k]; }
            }
            v2u us[4];
#pragma unroll
            for (int k = 0; k < 4; ++k) us[k] = *(LAS const v2u*)(uintptr_t)(S_U + ch * UP + (240 + 1024 * hf + 256 * k + 16 * r16 + 4 * g) * 2);
            WAIT_VM(0);
            LGKM0(); SB();
            lds_bump(C_Y);
            lds_wait(C_Y, 4);
            const float ds = ch ? bfhi(dsk2) : bflo(dsk2);
            unsigned ap[4][2];
#pragma unroll
            for (int k = 0; k < 4; ++k) {
                v4f yy = y[k] + ldsf4(ya + (hf ^ 1) * 256 + k * KCP);
#pragma unroll
                for (int h = 0; h < 2; ++h) {
                    unsigned yb = pk2(yy[2 * h], yy[2 * h + 1]);
                    unsigned sk = pk2(bflo(us[k][h]) * ds, bfhi(us[k][h]) * ds);
                    ap[k][h] = pk2(bflo(yb) + bflo(sk), bfhi(yb) + bfhi(sk));
                }
            }
            lds_bump(C_DONE);
            const unsigned vo = (g & 1) * 512 + r16 * 32 + (g >> 1) * 16;
#pragma unroll
            for (int k = 0; k < 4; k += 2) {
                auto s0 = __builtin_amdgcn_permlane16_swap(ap[k][0], ap[k + 1][0], false, false);
                auto s1 = __builtin_amdgcn_permlane16_swap(ap[k][1], ap[k + 1][1], false, false);
                st128(rs, (v4u){s0[0], s1[0], s0[1], s1[1]}, vo, WS_A + d * 4096 + hf * 2048 + k * 512);
            }
        }
        STAMP(22);
        lds_wait(C_H, 4);
        STAMP(23);
        issue_h();
        WAIT_VM(23);
        st_flag(rs, tag, WS_F3 + (hf * 512 + d) * FSTR);
        STAMP(24);
        preread();
    }
    v4f o[4][2];
#pragma unroll
    for (int tt = 0; tt < 4; ++tt) { o[tt][0] = (v4f){0, 0, 0, 0}; o[tt][1] = (v4f){0, 0, 0, 0}; }
    {
        const unsigned slotB = (kq < 2 ? kq * 11 + 7 : 14 + kq * 4) * 4096;
        unsigned wb[4];
#pragma unroll
        for (int tt = 0; tt < 4; ++tt) wb[tt] = (wda ^ (tt << 5)) + slotB;
#pragma unroll
        for (int i = 0; i < 11; ++i) {
            if (i < 10 || kq != 3) {
#pragma unroll
                for (int k = 0; k < 8; ++k) {
                    const int tt = k >> 1, n = (k ^ (k >> 1)) & 1;
                    o[tt][n] = MFMA16(fa[i][tt], __builtin_bit_cast(v8bf, fb[i][n]), o[tt][n]);
                    if (i >= 1 && i < 5 && (k & 1)) fa[6 + i][tt] = cat(ldstr(wb[tt] + (i - 1) * 4096), ldstr(wb[tt] + (i - 1) * 4096 + 512));
                    SB();
                }
            }
        }
    }
    STAMP(25);
    unsigned ra;
    if (role == 0) {
        poll(rs, r16 * FSTR, WS_F3 + ((im >> 4) * 512 + jd * 64 + 16 * lw) * FSTR, tag);
        const unsigned va = (lane >> 3) * 4096 + (lane & 7) * 16, sa = WS_A + (jd * 64 + 16 * lw) * 4096 + im * 128;
        v4u a0 = __builtin_amdgcn_raw_buffer_load_b128(rsb, va, sa, 16), a1 = __builtin_amdgcn_raw_buffer_load_b128(rsb, va, sa + 32768, 16);
        lds_wait(C_DONE, 4);
        ra = S_RG + lw * 8192 + lane * 16;
#pragma unroll
        for (int k = 0; k < 8; ++k) stsf4(ra + k * 1024, o[k >> 1][k & 1]);
        const unsigned ai = S_AI + (16 * lw + (lane >> 3)) * 128 + (((lane & 7) ^ (((lane >> 4) & 1) << 1)) << 4);
        stsu4(ai, a0); stsu4((ai ^ 64) + 1024, a1);
    } else {
        lds_wait(C_P0 + (kq & 1) * 4, 4);
        ra = slotA + mt * 8192 + lane * 16;
#pragma unroll
        for (int k = 0; k < 8; ++k) stsf4(ra + k * 1024, o[k >> 1][k & 1]);
    }
    LGKM0();
    STAMP(26);
    BARX();
    STAMP(27);
    {
        const unsigned pa = (lw >> 1) * 8192 + ((2 * role + (g >> 1)) * 2 + (lw & 1)) * 1024 + (g & 1) * 512 + r16 * 16;
        v4f s0 = ldsf4(S_RG + pa) + ldsf4(S_RG + 16384 + pa) + (ldsf4(pa) + ldsf4(11 * 4096 + pa));
        v4f s1 = ldsf4(S_RG + pa + 256) + ldsf4(S_RG + 16384 + pa + 256) + (ldsf4(pa + 256) + ldsf4(11 * 4096 + pa + 256));
        const unsigned aa = S_AI + role * 4096 + (wda ^ (lw << 5));
        v4u av = __builtin_bit_cast(v4u, cat(ldstr(aa), ldstr(aa + 512)));
        const float og = rbf(1.f - gate);
        unsigned bb[4] = {pk2(s0[0], s0[1]), pk2(s0[2], s0[3]), pk2(s1[0], s1[1]), pk2(s1[2], s1[3])};
        v4u ov;
#pragma unroll
        for (int k = 0; k < 4; ++k) {
            unsigned ga = pk2(gate * bflo(av[k]), gate * bfhi(av[k])), gb = pk2(og * bflo(bb[k]), og * bfhi(bb[k]));
            ov[k] = pk2(bflo(ga) + bflo(gb), bfhi(ga) + bfhi(gb));
        }
        *(v4u*)(A.out + (size_t)(im * 64 + 16 * lw + r16) * D_ + jd * 64 + 32 * role + 8 * g) = ov;
    }
    STAMP(28);
    STAMP_FLUSH(A.ws);
}

extern "C" void kernel_launch(void* const* d_in, const int* in_sizes, int n_in,
                              void* d_out, int out_size, void* d_ws, size_t ws_size,
                              hipStream_t stream) {
    Args a;
    a.x = (const u16*)d_in[0]; a.Wg = (const u16*)d_in[10]; a.Wu = (const u16*)d_in[11]; a.Wd = (const u16*)d_in[12];
    a.Are = (const u16*)d_in[1]; a.Aim = (const u16*)d_in[2]; a.Bre = (const u16*)d_in[3]; a.Bim = (const u16*)d_in[4];
    a.Cre = (const u16*)d_in[5]; a.Cim = (const u16*)d_in[6]; a.logdt = (const u16*)d_in[7]; a.Dskip = (const u16*)d_in[8];
    a.gw = (const u16*)d_in[13]; a.ws = (char*)d_ws; a.out = (u16*)d_out;
    (void)hipFuncSetAttribute((const void*)k_fused, hipFuncAttributeMaxDynamicSharedMemorySize, LDS_TOTAL);
    k_fused<<<256, 512, LDS_TOTAL, stream>>>(a);
}
```

```cpp
#define STAMPS
#define PROLE 1
#define PK0 0
#define PK1 0
#define PM0 0
#define PM1 0
#define PMULT 0
#include <hip/hip_runtime.h>
#include <hip/hip_bf16.h>
#include <stdint.h>

#define LAS __attribute__((address_space(3)))
typedef short  v4s  __attribute__((ext_vector_type(4)));
typedef short  v8s  __attribute__((ext_vector_type(8)));
typedef __bf16 v8bf __attribute__((ext_vector_type(8)));
typedef __bf16 v2bf __attribute__((ext_vector_type(2)));
typedef float  v2f  __attribute__((ext_vector_type(2)));
typedef float  v4f  __attribute__((ext_vector_type(4)));
typedef float  v16f __attribute__((ext_vector_type(16)));
typedef unsigned v4u __attribute__((ext_vector_type(4)));
typedef unsigned v2u __attribute__((ext_vector_type(2)));
typedef unsigned short u16;
typedef unsigned long long u64;

constexpr int L_ = 2048, D_ = 512, N_ = 128, F_ = 1376;
constexpr int NG = F_ / 16;

#define FSTR 128
constexpr unsigned WS_EP = 0;
constexpr unsigned WS_F1 = 4096;
constexpr unsigned WS_F2 = WS_F1 + 1024 * 128;
constexpr unsigned WS_F3 = WS_F2 + 1024 * 128;
constexpr unsigned WS_G  = WS_F3 + 1024 * 128;
constexpr unsigned WS_UT = WS_G + 8192;
constexpr unsigned WS_A  = WS_UT + 2048 * 512 * 2;
constexpr unsigned WS_H  = WS_A + 2048 * 512 * 2;
constexpr unsigned WS_END = WS_H + 64 * NG * 1024;

constexpr int X_RING = 0, X_SLOT = 16384, NXS = 3;
constexpr int WG_RING = X_RING + NXS * X_SLOT, W_SLOT = 6144, NWS = 5;
constexpr int WU_RING = WG_RING + NWS * W_SLOT;
constexpr int S_KC = 118784, KCP = 4160;
constexpr int S_U = S_KC + 8 * KCP, UP = 2288 * 2;
constexpr int TP = 272, S_TW = 32 * TP, S_T = S_U - 4 * S_TW;
constexpr int S_KD = S_U, KDN = 704;
constexpr int S_SYNC = S_KD + 4 * KDN * 4;
constexpr int C_P0 = S_SYNC, C_H = S_SYNC + 8, C_Y = S_SYNC + 12, C_DONE = S_SYNC + 16;
constexpr int S_RG = S_KC, S_AI = S_KC + 32768;
constexpr int LDS_TOTAL = 163840;
static_assert(S_T >= WU_RING + NWS * W_SLOT && S_SYNC + 32 <= LDS_TOTAL && S_AI + 8192 <= S_SYNC, "lds");

__device__ __forceinline__ float bflo(unsigned v) { return __uint_as_float(v << 16); }
__device__ __forceinline__ float bfhi(unsigned v) { return __uint_as_float(v & 0xffff0000u); }
__device__ __forceinline__ unsigned pk2(float lo, float hi) {
    v2f f = {lo, hi};
    return __builtin_bit_cast(unsigned, __builtin_convertvector(f, v2bf));
}
__device__ __forceinline__ float rbf(float x) { return bflo(pk2(x, x)); }

#define WAIT_VM(n)  asm volatile("s_waitcnt vmcnt(%0)" :: "n"(n) : "memory")
#define SB() __builtin_amdgcn_sched_barrier(0)
#define BARX() do { SB(); asm volatile("s_barrier" ::: "memory"); SB(); } while (0)
#define LGKM0() __builtin_amdgcn_s_waitcnt(0xC07F)

constexpr size_t WS_STAMP = (size_t)200 << 20;
#define STAMP(k) do { unsigned long long t_, c_; asm volatile("s_memrealtime %0\n\ts_memtime %1\n\ts_waitcnt lgkmcnt(0)" : "=&s"(t_), "=&s"(c_) :: "memory"); \
    asm volatile("v_writelane_b32 %0, %1, %3\n\tv_writelane_b32 %0, %2, %4" : "+v"(stampv) : "s"((unsigned)t_), "s"((unsigned)c_), "n"(k), "n"(32 + (k))); } while (0)
#define STAMP_FLUSH(ws) do { ((unsigned*)((char*)(ws) + WS_STAMP))[(blockIdx.x * 8 + w) * 64 + lane] = stampv; } while (0)

typedef v4u rsrc_t;
__device__ __forceinline__ rsrc_t mkrsrc(const void* p, unsigned bytes) {
    uint64_t a = (uint64_t)p;
    rsrc_t r = {(unsigned)a, (unsigned)(a >> 32) & 0xffffu, bytes, 0x00020000u};
    return r;
}
__device__ __forceinline__ void dma1(rsrc_t r, unsigned m0v, unsigned v0, unsigned soff) {
    asm volatile("s_mov_b32 m0, %0\n\ts_nop 0\n\tbuffer_load_dwordx4 %1, %2, %3 offen lds"
                 :: "s"(m0v), "v"(v0), "s"(r), "s"(soff) : "memory");
}
__device__ __forceinline__ void dma2(rsrc_t r, unsigned m0v, unsigned v0, unsigned v1, unsigned soff) {
    asm volatile("s_mov_b32 m0, %0\n\ts_nop 0\n\tbuffer_load_dwordx4 %1, %3, %4 offen lds\n\tbuffer_load_dwordx4 %2, %3, %4 offen offset:1024 lds"
                 :: "s"(m0v), "v"(v0), "v"(v1), "s"(r), "s"(soff) : "memory");
}
__device__ __forceinline__ void dma3(rsrc_t r, unsigned m0v, unsigned v0, unsigned v1, unsigned v2, unsigned soff) {
    asm volatile("s_mov_b32 m0, %0\n\ts_nop 0\n\tbuffer_load_dwordx4 %1, %4, %5 offen lds\n\tbuffer_load_dwordx4 %2, %4, %5 offen offset:1024 lds"
                 "\n\tbuffer_load_dwordx4 %3, %4, %5 offen offset:2048 lds"
                 :: "s"(m0v), "v"(v0), "v"(v1), "v"(v2), "s"(r), "s"(soff) : "memory");
}
__device__ __forceinline__ void st128(rsrc_t r, v4u d, unsigned voff, unsigned soff) {
    asm volatile("buffer_store_dwordx4 %0, %1, %2, %3 offen sc1\n\ts_nop 1" :: "v"(d), "v"(voff), "s"(r), "s"(soff) : "memory");
}
__device__ __forceinline__ void st32(rsrc_t r, unsigned d, unsigned voff, unsigned soff) {
    asm volatile("buffer_store_dword %0, %1, %2, %3 offen sc1" :: "v"(d), "v"(voff), "s"(r), "s"(soff) : "memory");
}
__device__ __forceinline__ void st_flag(rsrc_t r, u64 tag, unsigned soff) {
    v2u d = {(unsigned)tag, (unsigned)(tag >> 32)};
    u64 sv;
    asm volatile("s_mov_b64 %0, exec\n\ts_mov_b64 exec, 1\n\tbuffer_store_dwordx2 %1, off, %2, %3 sc1\n\ts_mov_b64 exec, %0"
                 : "=&s"(sv) : "v"(d), "s"(r), "s"(soff) : "memory");
}
__device__ __forceinline__ void ld64_sc1(v2u& d, rsrc_t r, unsigned voff, unsigned soff) {
    asm volatile("buffer_load_dwordx2 %0, %1, %2, %3 offen sc1" : "=v"(d) : "v"(voff), "s"(r), "s"(soff) : "memory");
}
__device__ __forceinline__ void ld128_sc1(v4u& d, rsrc_t r, unsigned voff, unsigned soff) {
    asm volatile("buffer_load_dwordx4 %0, %1, %2, %3 offen sc1" : "=v"(d) : "v"(voff), "s"(r), "s"(soff) : "memory");
}
__device__ __forceinline__ bool tag_ok(v2u f, u64 tag) {
    return __builtin_amdgcn_ballot_w64((((u64)f[1] << 32) | f[0]) != tag) == 0;
}
constexpr int SPIN_MAX = 1 << 18;
__device__ __forceinline__ void poll(rsrc_t r, unsigned voff, unsigned soff, u64 tag) {
#pragma unroll 1
    for (int i = 0; i < SPIN_MAX; ++i) {
        v2u f;
        asm volatile("buffer_load_dwordx2 %0, %1, %2, %3 offen sc1\n\ts_waitcnt vmcnt(0)" : "=&v"(f) : "v"(voff), "s"(r), "s"(soff) : "memory");
        if (tag_ok(f, tag)) break;
        __builtin_amdgcn_s_sleep(1);
    }
}
__device__ __forceinline__ void lds_bump(unsigned addr) {
    u64 sv;
    asm volatile("s_mov_b64 %0, exec\n\ts_mov_b64 exec, 1\n\tds_add_u32 %1, %2\n\ts_mov_b64 exec, %0" : "=&s"(sv) : "v"(addr), "v"(1u) : "memory");
}
__device__ __forceinline__ void lds_wait(unsigned addr, unsigned n) {
#pragma unroll 1
    for (int i = 0; i < SPIN_MAX * 8; ++i) {
        unsigned v;
        asm volatile("ds_read_b32 %0, %1\n\ts_waitcnt lgkmcnt(0)" : "=&v"(v) : "v"(addr) : "memory");
        if (__builtin_amdgcn_readfirstlane(v) >= n) break;
    }
    SB();
}

__device__ __forceinline__ v8bf ldsb128(unsigned a) { return *(LAS const v8bf*)(uintptr_t)a; }
__device__ __forceinline__ v4f ldsf4(unsigned a) { return *(LAS const v4f*)(uintptr_t)a; }
__device__ __forceinline__ void stsf4(unsigned a, v4f v) { *(LAS v4f*)(uintptr_t)a = v; }
__device__ __forceinline__ void stsu4(unsigned a, v4u v) { *(LAS v4u*)(uintptr_t)a = v; }
__device__ __forceinline__ void stsu1(unsigned a, unsigned v) { *(LAS unsigned*)(uintptr_t)a = v; }
__device__ __forceinline__ v4s ldstr(unsigned a) { return __builtin_amdgcn_ds_read_tr16_b64_v4i16((LAS v4s*)(uintptr_t)a); }
__device__ __forceinline__ v8bf cat(v4s a, v4s b) { return __builtin_bit_cast(v8bf, __builtin_shufflevector(a, b, 0, 1, 2, 3, 4, 5, 6, 7)); }
#define MFMA32(a, b, c) __builtin_amdgcn_mfma_f32_32x32x16_bf16((a), (b), (c), 0, 0, 0)
#define MFMA16(a, b, c) __builtin_amdgcn_mfma_f32_16x16x32_bf16((a), (b), (c), 0, 0, 0)

struct cf { float r, i; };
__device__ __forceinline__ cf cmul(cf a, cf b) { return {a.r * b.r - a.i * b.i, a.r * b.i + a.i * b.r}; }
__device__ __forceinline__ cf cexp_(float al, float be, float e) {
    float m = __builtin_amdgcn_exp2f(al * e);
    float th = __builtin_amdgcn_fractf(be * e);
    return {m * __builtin_amdgcn_cosf(th), m * __builtin_amdgcn_sinf(th)};
}

struct G1Frag { v8bf b[4]; v8bf a[6]; };
template <bool RD, bool MM>
__device__ __forceinline__ void g1_step(v4f (&acc)[6][4], const G1Frag& cur, G1Frag& nxt, unsigned xa, unsigned wa0, unsigned wa1, bool full) {
    auto rdA = [&](int a) {
        const unsigned p = ((a >> 1) == 1 ? wa1 : wa0) + (a >> 1) * 32 + (a & 1) * (WU_RING - WG_RING);
        nxt.a[a] = cat(ldstr(p), ldstr(p + 4 * 192));
    };
#pragma unroll
    for (int i = 0; i < 16; ++i) {
        const int a = i >> 2, n = (a & 1) ? 3 - (i & 3) : (i & 3);
        if (MM) acc[a][n] = MFMA16(cur.a[a], cur.b[n], acc[a][n]);
        if (RD) {
            if (i < 4) nxt.b[i] = ldsb128(xa + i * 2048);
            else if (!(i & 1)) rdA((i - 4) >> 1);
        }
        SB();
    }
    if (MM && full) {
#pragma unroll
        for (int i = 16; i < 24; ++i) {
            const int a = i >> 2, n = (a & 1) ? 3 - (i & 3) : (i & 3);
            acc[a][n] = MFMA16(cur.a[a], cur.b[n], acc[a][n]);
            SB();
        }
    }
}

struct Args {
    const u16 *x, *Wg, *Wu, *Wd, *Are, *Aim, *Bre, *Bim, *Cre, *Cim, *logdt, *Dskip, *gw;
    char* ws; u16* out;
};

__global__ __launch_bounds__(512, 2) void k_fused(Args A) {
    const int t = threadIdx.x, lane = t & 63, w = __builtin_amdgcn_readfirstlane(t >> 6);
    const int b = blockIdx.x;
    const int xcd = b & 7, c = b >> 3;
    const int pi = (xcd & 1) * 8 + (c & 7), tq = xcd >> 1, tr = c >> 3, tj = tq * 4 + tr;
    const bool full = !(tr == 3 || tj == 14);
    const int sp = tq * 11 + 3 * tr - (tj == 15);
    const int im = xcd * 4 + (c >> 3), jd = c & 7;
    const int lw = w & 3, role = w >> 2;
    unsigned stampv = 0;
    STAMP(0);
    const unsigned vx0 = (lane >> 3) * 1024 + (((lane & 7) ^ (lane >> 4)) << 4);
    const unsigned vx1 = (vx0 ^ 64) + 8192 - 1024;
    unsigned vw[3];
#pragma unroll
    for (int i = 0; i < 3; ++i) {
        int cc = 64 * i + lane, row = cc / 12, ch = (cc % 12) ^ ((row >> 3) << 1);
        int src = ch;
        if (!full) src = ch < 6 ? ch : ch - 2;
        vw[i] = row * (F_ * 2) + src * 16 - 1024 * i;
        if (!full && (ch == 4 || ch == 5 || ch >= 10)) vw[i] = 0x7f000000u;
    }
    const rsrc_t rx = mkrsrc(A.x, L_ * D_ * 2), rw = mkrsrc(lw >> 1 ? A.Wu : A.Wg, D_ * F_ * 2);
    const unsigned sx0 = (pi * 128 + 16 * lw) * 1024, sw0 = (lw & 1) * 16 * (F_ * 2) + sp * 64;
    const unsigned mx0 = X_RING + lw * 2048, mw0 = (lw >> 1 ? WU_RING : WG_RING) + (lw & 1) * 3072;
    auto ldx = [&](int u, int h) { dma2(rx, mx0 + (u % NXS) * X_SLOT + h * 8192, vx0, vx1, sx0 + h * 65536 + u * 128); };
    auto ldw = [&](int s) { dma3(rw, mw0 + (s % NWS) * W_SLOT, vw[0], vw[1], vw[2], sw0 + s * 32 * (F_ * 2)); };
    dma2(rx, mx0 + role * 8192, vx0, vx1, sx0 + role * 65536);
    dma3(rw, mw0 + role * W_SLOT, vw[0], vw[1], vw[2], sw0 + role * 32 * (F_ * 2));
    STAMP(1);
    const rsrc_t rs = mkrsrc(A.ws, WS_END), rd = mkrsrc(A.Wd, F_ * D_ * 2);
    u64 tag = *(const __attribute__((address_space(4))) u64*)(uintptr_t)(A.ws + WS_EP) + 1;
    const int q = (lane >> 2) & 3, p = lane & 3, g = lane >> 4, r16 = lane & 15;
    const int kq = (role << 1) | (lw >> 1), mt = lw & 1;
    const unsigned wda = (8 * g + q) * 128 + p * 8 + (((q >> 1) | ((g & 1) << 1)) << 5);
    const unsigned slotA = (kq & 1) * 11 * 4096;
    v8bf fa[11][4];
    v4u fb[11][2];
    float gate;
    const __amdgpu_buffer_rsrc_t rsb = __builtin_amdgcn_make_buffer_rsrc((void*)A.ws, 0, WS_END, 0x00020000);
    auto issue_h = [&]() {
        const unsigned vh = (g >> 1) * 1024 + ((g & 1) * 32 + r16) * 16;
        const unsigned sh = WS_H + ((2 * im + mt) * NG + 22 * kq) * 1024;
#pragma unroll
        for (int i = 0; i < 10; ++i) {
            fb[i][0] = __builtin_amdgcn_raw_buffer_load_b128(rsb, vh, sh + i * 2048, 16);
            fb[i][1] = __builtin_amdgcn_raw_buffer_load_b128(rsb, vh + 256, sh + i * 2048, 16);
        }
        const unsigned vl = kq == 3 ? 0x7f000000u : vh;
        fb[10][0] = __builtin_amdgcn_raw_buffer_load_b128(rsb, vl, sh + 10 * 2048, 16);
        fb[10][1] = __builtin_amdgcn_raw_buffer_load_b128(rsb, vl + 256, sh + 10 * 2048, 16);
        gate = __uint_as_float(__builtin_amdgcn_raw_buffer_load_b32(rsb, r16 * 4, WS_G + (im * 64 + 16 * lw) * 4, 16));
        SB();
    };
    auto preread = [&]() {
#pragma unroll
        for (int i = 0; i < 7; ++i)
#pragma unroll
            for (int tt = 0; tt < 4; ++tt) {
                const unsigned a = ((wda ^ (tt << 5)) + slotA) + i * 4096;
                fa[i][tt] = cat(ldstr(a), ldstr(a + 512));
            }
        LGKM0(); SB();
        lds_bump(C_P0 + (kq & 1) * 4);
    };

    if (w < 4) {
        __builtin_amdgcn_s_setprio(1);
        const int wf = w & 1, wm = w >> 1;
        const unsigned xoff = X_RING + (64 * wm + r16) * 128 + ((g ^ (r16 >> 1)) << 4);
        const unsigned wbase = WG_RING + (8 * g + q) * 192 + p * 8 + wf * 96;
        const unsigned sgn = (g & 1) * 32;
        const unsigned w0 = wf ? wbase - sgn : wbase + sgn, w1 = wf ? wbase + sgn : wbase - sgn;
        v4f acc[6][4];
#pragma unroll
        for (int j = 0; j < 6; ++j)
#pragma unroll
            for (int n = 0; n < 4; ++n) { acc[j][n] = (v4f){0, 0, 0, 0}; asm volatile("" : "+v"(acc[j][n])); }
        G1Frag fA, fB;
        asm volatile("" :: "s"(tag));
        WAIT_VM(0);
        BARX();
        g1_step<true, false>(acc, fB, fA, xoff, w0, w1, full);
        unsigned xs = 0, wsl = W_SLOT;
#pragma unroll 1
        for (int u = 0; u < 7; ++u) {
            LGKM0(); BARX();
            {
                const unsigned xa = (xoff ^ 64) + xs, a0 = w0 + wsl, a1 = w1 + wsl;
                wsl += W_SLOT; if (wsl == NWS * W_SLOT) wsl = 0;
                xs += X_SLOT; if (xs == NXS * X_SLOT) xs = 0;
                g1_step<true, true>(acc, fA, fB, xa, a0, a1, full);
            }
            LGKM0(); BARX();
            {
                const unsigned xa = xoff + xs, a0 = w0 + wsl, a1 = w1 + wsl;
                wsl += W_SLOT; if (wsl == NWS * W_SLOT) wsl = 0;
                g1_step<true, true>(acc, fB, fA, xa, a0, a1, full);
            }
        }
        LGKM0(); BARX();
        g1_step<true, true>(acc, fA, fB, (xoff ^ 64) + (7 % NXS) * X_SLOT, w0 + (15 % NWS) * W_SLOT, w1 + (15 % NWS) * W_SLOT, full);
        LGKM0(); BARX();
        g1_step<false, true>(acc, fB, fA, 0, 0, 0, full);
        STAMP(2);
        const float c1 = -2.f * 0.7978845608028654f * 1.44269504088896341f, c2 = c1 * 0.044715f;
        const unsigned sh = WS_H + ((pi * 4 + 2 * wm) * NG + 2 * sp + (full ? 3 : 2) * wf) * 1024;
#pragma unroll
        for (int j = 0; j < 3; ++j) {
            if (j < 2 || full) {
#pragma unroll
                for (int m2 = 0; m2 < 2; ++m2) {
                    unsigned hp[2][2];
#pragma unroll
                    for (int n = 0; n < 2; ++n)
#pragma unroll
                        for (int h = 0; h < 2; ++h) {
                            v2f gg = {acc[2 * j][2 * m2 + n][2 * h], acc[2 * j][2 * m2 + n][2 * h + 1]};
                            v2f uu = {acc[2 * j + 1][2 * m2 + n][2 * h], acc[2 * j + 1][2 * m2 + n][2 * h + 1]};
                            v2f ee = gg * (gg * gg * c2 + c1);
                            v2f dd = (v2f){__builtin_amdgcn_exp2f(ee[0]), __builtin_amdgcn_exp2f(ee[1])} + 1.f;
                            v2f hh = gg * uu * (v2f){__builtin_amdgcn_rcpf(dd[0]), __builtin_amdgcn_rcpf(dd[1])};
                            hp[n][h] = pk2(hh[0], hh[1]);
                        }
                    auto s0 = __builtin_amdgcn_permlane16_swap(hp[0][0], hp[1][0], false, false);
                    auto s1 = __builtin_amdgcn_permlane16_swap(hp[0][1], hp[1][1], false, false);
                    st128(rs, (v4u){s0[0], s1[0], s0[1], s1[1]}, lane * 16, sh + (m2 * NG + j) * 1024);
                }
            }
        }
        STAMP(3);
        BARX();
        STAMP(4);
        preread();
        lds_wait(C_P0 + (kq & 1) * 4, 2);
        STAMP(5);
        {
            const unsigned v0 = (lane >> 3) * 1024 + (((lane & 7) ^ (((lane >> 4) & 1) << 1)) << 4);
            const unsigned v1 = (v0 ^ 64) + 8192 - 1024;
            const unsigned s0 = ((kq + 2) * 11 * 32 + 16 * mt) * 1024 + jd * 128;
#pragma unroll
            for (int i = 0; i < 7; ++i) dma2(rd, slotA + i * 4096 + mt * 2048, v0, v1, s0 + i * 32768);
        }
        WAIT_VM(14);
        st_flag(rs, tag, WS_F2 + ((2 * pi + wm) * 32 + 2 * tj + wf) * FSTR);
        STAMP(6);
        poll(rs, (lane & 31) * FSTR, WS_F2 + im * 32 * FSTR, tag);
        STAMP(7);
        issue_h();
        lds_bump(C_H);
        __builtin_amdgcn_s_setprio(0);
    } else {
        const int ch = lw >> 1, hf = lw & 1, d = 2 * b + ch, st = t - 256;
        unsigned pr[6];
        {
            const unsigned po = (d * N_ + 64 * hf + lane) * 2;
            const u16* ps[6] = {A.Are, A.Aim, A.Bre, A.Bim, A.Cre, A.Cim};
#pragma unroll
            for (int i = 0; i < 6; ++i) asm volatile("global_load_ushort %0, %1, %2" : "=v"(pr[i]) : "v"(po), "s"(ps[i]) : "memory");
        }
        ldx(1, 0); ldx(1, 1); ldw(2);
        const unsigned ldt2 = *(const __attribute__((address_space(4))) unsigned*)(uintptr_t)(A.logdt + 2 * b);
        const unsigned dsk2 = *(const __attribute__((address_space(4))) unsigned*)(uintptr_t)(A.Dskip + 2 * b);
        if (st < 8) stsu1(S_SYNC + st * 4, 0);
        asm volatile("" :: "s"(tag), "s"(ldt2), "s"(dsk2));
        LGKM0();
        WAIT_VM(16);
        BARX();
        STAMP(2);
        WAIT_VM(13);
        BARX();
        STAMP(3);
        ldw(3);
        dma2(rx, S_KD + lw * 2048, vx0, vx1, sx0 + (tj & 1) * 65536 + (tj >> 1) * 128);
        v4u gx0, gx1, gw0, gw1;
        {
            const unsigned go = (pi * 128 + 8 * tj + 2 * lw + (lane >> 5)) * 1024 + (lane & 31) * 32, wo = (lane & 31) * 32;
            asm volatile("global_load_dwordx4 %0, %1, %2" : "=v"(gx0) : "v"(go), "s"(A.x) : "memory");
            asm volatile("global_load_dwordx4 %0, %1, %2 offset:16" : "=v"(gx1) : "v"(go), "s"(A.x) : "memory");
            asm volatile("global_load_dwordx4 %0, %1, %2" : "=v"(gw0) : "v"(wo), "s"(A.gw) : "memory");
            asm volatile("global_load_dwordx4 %0, %1, %2 offset:16" : "=v"(gw1) : "v"(wo), "s"(A.gw) : "memory");
        }
        ldx(2, 0);
        WAIT_VM(11);
        BARX();
        STAMP(4);
        asm volatile("" : "+v"(pr[0]), "+v"(pr[1]), "+v"(pr[2]), "+v"(pr[3]), "+v"(pr[4]), "+v"(pr[5]));
        ldx(2, 1); ldw(4);
        const float dt = rbf(__expf(ch ? bfhi(ldt2) : bflo(ldt2)));
        const float al = dt * bflo(pr[0]) * 1.44269504088896341f, be = dt * bflo(pr[1]) * 0.15915494309189535f;
        cf wv = cmul(cf{bflo(pr[4]), bflo(pr[5])}, cf{dt * bflo(pr[2]), dt * bflo(pr[3])});
        cf z1 = cexp_(al, be, 1.f), z8 = cexp_(al, be, 8.f), z256 = cexp_(al, be, 256.f);
        cf z16 = cmul(z8, z8);
        asm volatile("" : "+v"(wv.r), "+v"(wv.i), "+v"(z1.r), "+v"(z1.i), "+v"(z8.r), "+v"(z8.i), "+v"(z16.r), "+v"(z16.i), "+v"(z256.r), "+v"(z256.i));
        ldw(5); ldx(3, 0);
        WAIT_VM(12);
        BARX();
        STAMP(5);
        {
            v4u o[2];
#pragma unroll
            for (int i = 0; i < 2; ++i) {
                const int r0 = 32 * i + 8 * g + q, r1 = r0 + 4;
                const unsigned a0 = S_KD + r0 * 128 + (((2 * lw + (p >> 1)) ^ ((r0 >> 1) & 7)) << 4) + (p & 1) * 8;
                const unsigned a1 = S_KD + r1 * 128 + (((2 * lw + (p >> 1)) ^ ((r1 >> 1) & 7)) << 4) + (p & 1) * 8;
                o[i] = __builtin_bit_cast(v4u, cat(ldstr(a0), ldstr(a1)));
            }
            const unsigned vo = (r16 >> 1) * 8192 + (r16 & 1) * 64 + g * 16;
            const unsigned so = WS_UT + ((tj >> 1) * 32 + 8 * lw) * 8192 + (4 * pi + 2 * (tj & 1)) * 128;
            st128(rs, o[0], vo, so); st128(rs, o[1], vo, so + 128);
            asm volatile("" : "+v"(gx0), "+v"(gx1), "+v"(gw0), "+v"(gw1));
            float s = 0.f;
#pragma unroll
            for (int k = 0; k < 4; ++k) {
                unsigned p0 = pk2(bflo(gx0[k]) * bflo(gw0[k]), bfhi(gx0[k]) * bfhi(gw0[k]));
                unsigned p1 = pk2(bflo(gx1[k]) * bflo(gw1[k]), bfhi(gx1[k]) * bfhi(gw1[k]));
                s += bflo(p0) + bfhi(p0) + bflo(p1) + bfhi(p1);
            }
#pragma unroll
            for (int m = 16; m >= 1; m >>= 1) s += __shfl_xor(s, m);
            s = rbf(s);
            float gg = rbf(__builtin_amdgcn_rcpf(1.f + __expf(-s)));
            st32(rs, __float_as_uint(gg), (lane & 31) ? 0x7f000000u : (lane >> 5) * 4, WS_G + (pi * 128 + 8 * tj + 2 * lw) * 4);
        }
        ldx(3, 1); ldw(6);
        WAIT_VM(13);
        BARX();
        STAMP(6);
        const unsigned T = S_T + lw * S_TW + lane * 4;
        cf e = {1.f, 0.f}, o = z8;
        auto e1rows = [&](int r0, int r1) {
#pragma unroll
            for (int r = r0; r < r1; r += 2) {
                stsu1(T + r * TP, pk2(e.r, e.i));
                stsu1(T + (r + 1) * TP, pk2(o.r, o.i));
                if (r + 2 < 32) { e = cmul(e, z16); o = cmul(o, z16); }
            }
            asm volatile("" : "+v"(e.r), "+v"(e.i), "+v"(o.r), "+v"(o.i));
        };
        e1rows(0, 10);
        ldw(7); ldx(4, 0); ldw(8);
        WAIT_VM(13);
        BARX();
        STAMP(7);
        st_flag(rs, tag, WS_F1 + (((tj >> 1) * 4 + lw) * 32 + 2 * pi + (tj & 1)) * FSTR);
        e1rows(10, 22);
        ldx(4, 1); ldw(9);
        WAIT_VM(14);
        BARX();
        STAMP(8);
        e1rows(22, 32);
        ldx(5, 0); ldw(10);
        WAIT_VM(16);
        BARX();
        STAMP(9);
        v8bf ka[8];
        {
            const unsigned TA = S_T + lw * S_TW + (lane & 31) * TP + (lane >> 5) * 16;
#pragma unroll
            for (int s = 0; s < 8; ++s) ka[s] = ldsb128(TA + s * 32);
            unsigned ev[8];
            cf pp = wv;
#pragma unroll
            for (int k = 0; k < 8; ++k) { ev[k] = pk2(pp.r, -pp.i); if (k < 7) pp = cmul(pp, z1); }
            LGKM0(); SB();
#pragma unroll
            for (int k = 0; k < 8; ++k) stsu1(T + k * TP, ev[k]);
        }
        ldx(5, 1); ldw(11);
        WAIT_VM(13);
        BARX();
        STAMP(10);
        {
            const cf z2 = cmul(z1, z1), z4 = cmul(z2, z2);
            cf qq = cmul(wv, z256);
            cf q2 = qq;
#pragma unroll
            for (int k = 0; k < 4; ++k) { stsu1(T + (8 + k) * TP, pk2(q2.r, -q2.i)); if (k < 3) q2 = cmul(q2, z2); }
#pragma unroll
            for (int k = 0; k < 2; ++k) {
                qq = cmul(qq, z256);
                cf q4 = cmul(qq, z4);
                stsu1(T + (12 + 2 * k) * TP, pk2(qq.r, -qq.i));
                stsu1(T + (13 + 2 * k) * TP, pk2(q4.r, -q4.i));
            }
#pragma unroll
            for (int k = 0; k < 5; ++k) { qq = cmul(qq, z256); stsu1(T + (16 + k) * TP, pk2(qq.r, -qq.i)); }
        }
        ldx(6, 0); ldw(12);
        WAIT_VM(15);
        BARX();
        STAMP(11);
        {
            const int r = lane & 31, hh = lane >> 5;
            const unsigned TB = S_T + lw * S_TW + (r < 20 ? r : 20) * TP + hh * 16;
            v16f kacc = {};
#pragma unroll
            for (int s = 0; s < 8; ++s) kacc = MFMA32(ka[s], ldsb128(TB + s * 32), kacc);
            int base, stride;
            if (r < 8) { base = r; stride = 8; }
            else if (r < 12) { base = 256 + (r - 8); stride = 4; }
            else if (r < 16) { base = 384 + 64 * ((r - 12) >> 1) + ((r - 12) & 1); stride = 2; }
            else if (r <= 20) { base = 512 + 32 * (r - 16); stride = 1; }
            else { base = 672; stride = 1; }
            const unsigned KD = S_KD + lw * KDN * 4 + (base + stride * 4 * hh) * 4;
#pragma unroll
            for (int k = 0; k < 16; ++k) stsu1(KD + stride * ((k & 3) + 8 * (k >> 2)) * 4, __float_as_uint(kacc[k]));
        }
        ldx(6, 1); ldw(13);
        LGKM0();
        WAIT_VM(13);
        BARX();
        STAMP(12);
        unsigned kidx[12];
#pragma unroll
        for (int k = 0; k < 12; ++k) {
            int j = 2047 - (8 * st + k);
            int jj = j < 0 ? 0 : j;
            kidx[k] = S_KD + (jj < 128 ? jj : (int)(pk2((float)jj, 0.f) & 0xffffu) - 17024) * 4;
        }
#pragma unroll
        for (int k = 0; k < 12; ++k) asm volatile("" : "+v"(kidx[k]));
        ldx(7, 0); ldw(14);
        WAIT_VM(15);
        BARX();
        STAMP(13);
        auto expand = [&](int cc) {
            float v[12];
#pragma unroll
            for (int k = 0; k < 11; ++k) {
                float x = *(LAS const float*)(uintptr_t)(kidx[k] + 2 * cc * KDN * 4) + *(LAS const float*)(uintptr_t)(kidx[k] + (2 * cc + 1) * KDN * 4);
                v[k] = (k >= 8 && st == 255) ? 0.f : x;
            }
#pragma unroll
            for (int s = 0; s < 4; ++s) {
                v4u oo = {pk2(v[s], v[s + 1]), pk2(v[s + 2], v[s + 3]), pk2(v[s + 4], v[s + 5]), pk2(v[s + 6], v[s + 7])};
                stsu4(S_KC + (cc * 4 + s) * KCP + st * 16, oo);
            }
        };
        v2u f1;
        const unsigned vf1 = (lane & 31) * FSTR, sf1 = WS_F1 + (b >> 3) * 32 * FSTR;
        ld64_sc1(f1, rs, vf1, sf1);
        expand(0);
        ldx(7, 1); ldw(15);
        WAIT_VM(14);
        BARX();
        STAMP(14);
        expand(1);
        if (st < 16) stsu4(S_KC + (st >> 1) * KCP + 4096 + (st & 1) * 16, (v4u){0, 0, 0, 0});
        LGKM0();
        WAIT_VM(11);
        BARX();
        STAMP(15);
        if (st < 60) stsu4(S_U + (st >= 30 ? UP : 0) + (st % 30) * 16, (v4u){0, 0, 0, 0});
        WAIT_VM(3);
        BARX();
        STAMP(16);
        asm volatile("" : "+v"(f1));
        if (!tag_ok(f1, tag)) poll(rs, vf1, sf1, tag);
        v4u uu0, uu1;
        ld128_sc1(uu0, rs, st * 16, WS_UT + b * 8192); ld128_sc1(uu1, rs, st * 16, WS_UT + b * 8192 + 4096);
        WAIT_VM(2);
        BARX();
        STAMP(17);
        if (b == 0 && lw == 3) {
#pragma unroll 1
            for (int k = 0; k < 4; ++k) poll(rs, ((lane >> 5) * 128 + (lane & 31)) * FSTR, WS_F1 + k * 256 * FSTR, tag);
            st_flag(rs, tag, WS_EP);
        }
        BARX();
        STAMP(18);
        const unsigned vwd = (lane >> 3) * 1024 + (((lane & 7) ^ ((((lane >> 4) & 1) | ((lw & 1) << 1)) << 1)) << 4);
        {
            const unsigned s0 = lw * 8192 + jd * 128;
#pragma unroll 1
            for (int i = 0; i < 22; ++i) dma1(rd, i * 4096 + lw * 1024, vwd, s0 + i * 32768);
#pragma unroll
            for (int i = 22; i < 29; ++i) dma1(rd, i * 4096 + lw * 1024, vwd, s0 + (i < 26 ? i + 7 : i + 14) * 32768);
        }
        WAIT_VM(7);
        asm volatile("" : "+v"(uu0), "+v"(uu1));
        {
            const unsigned ua = S_U + ((st >> 2) & 1) * UP + (240 + 32 * (st >> 3) + 8 * (st & 3)) * 2;
            stsu4(ua, uu0); stsu4(ua + 2048, uu1);
        }
        LGKM0();
        STAMP(19);
        BARX();
        STAMP(20);
        v4f y[8];
#pragma unroll
        for (int k = 0; k < 8; ++k) y[k] = (v4f){0, 0, 0, 0};
        {
            const int rp = 15 - r16;
            unsigned KA = S_KC + (ch * 4 + (rp & 3)) * KCP + (8 * g + (rp & 12) + 128 * hf) * 2;
            unsigned UB = S_U + ch * UP + (16 * r16 + 8 * g + 128 * hf) * 2;
            v2u al_[2][8], ah_[2][8]; v8bf bb[2][8];
            auto rdm = [&](int buf, int m, int off) {
                asm volatile("ds_read_b64 %0, %1 offset:%2" : "=v"(al_[buf][m]) : "v"(KA), "n"(off + (7 - m) * 512) : "memory");
                asm volatile("ds_read_b64 %0, %1 offset:%2" : "=v"(ah_[buf][m]) : "v"(KA), "n"(off + (7 - m) * 512 + 8) : "memory");
                asm volatile("ds_read_b128 %0, %1 offset:%2" : "=v"(bb[buf][m]) : "v"(UB), "n"(off + m * 512) : "memory");
            };
#pragma unroll
            for (int m = 0; m < 8; ++m) rdm(0, m, 0);
#pragma unroll
            for (int tt = 0; tt < 4; ++tt) {
                const int cb = tt & 1;
                asm volatile("s_waitcnt lgkmcnt(0)" ::: "memory");
#pragma unroll
                for (int m = 0; m < 8; ++m) asm volatile("" : "+v"(al_[cb][m]), "+v"(ah_[cb][m]), "+v"(bb[cb][m]));
                SB();
                int cnt = 0;
#pragma unroll
                for (int m = 0; m < 8; ++m)
#pragma unroll
                    for (int k = 0; k + m < 8; ++k) {
                        v4u x = {al_[cb][m][0], al_[cb][m][1], ah_[cb][m][0], ah_[cb][m][1]};
                        y[m + k] = MFMA16(__builtin_bit_cast(v8bf, x), bb[cb][k], y[m + k]);
                        if (tt < 3 && (cnt % 3) == 0 && cnt < 24) rdm(cb ^ 1, cnt / 3, (tt + 1) * 64);
                        ++cnt;
                        SB();
                    }
            }
        }
        STAMP(21);
        {
            const unsigned ya = S_KC + ch * 4 * KCP + (lane >> 3) * 512 + 128 + (lane & 7) * 16;
            if (hf == 0) {
#pragma unroll
                for (int k = 0; k < 4; ++k) stsf4(ya + k * KCP, y[4 + k]);
            } else {
#pragma unroll
                for (int k = 0; k < 4; ++k) { stsf4(ya + 256 + k * KCP, y[k]); y[k] = y[4 + k]; }
            }
            v2u us[4];
#pragma unroll
            for (int k = 0; k < 4; ++k) us[k] = *(LAS const v2u*)(uintptr_t)(S_U + ch * UP + (240 + 1024 * hf + 256 * k + 16 * r16 + 4 * g) * 2);
            WAIT_VM(0);
            LGKM0(); SB();
            lds_bump(C_Y);
            lds_wait(C_Y, 4);
            const float ds = ch ? bfhi(dsk2) : bflo(dsk2);
            unsigned ap[4][2];
#pragma unroll
            for (int k = 0; k < 4; ++k) {
                v4f yy = y[k] + ldsf4(ya + (hf ^ 1) * 256 + k * KCP);
#pragma unroll
                for (int h = 0; h < 2; ++h) {
                    unsigned yb = pk2(yy[2 * h], yy[2 * h + 1]);
                    unsigned sk = pk2(bflo(us[k][h]) * ds, bfhi(us[k][h]) * ds);
                    ap[k][h] = pk2(bflo(yb) + bflo(sk), bfhi(yb) + bfhi(sk));
                }
            }
            lds_bump(C_DONE);
            const unsigned vo = (g & 1) * 512 + r16 * 32 + (g >> 1) * 16;
#pragma unroll
            for (int k = 0; k < 4; k += 2) {
                auto s0 = __builtin_amdgcn_permlane16_swap(ap[k][0], ap[k + 1][0], false, false);
                auto s1 = __builtin_amdgcn_permlane16_swap(ap[k][1], ap[k + 1][1], false, false);
                st128(rs, (v4u){s0[0], s1[0], s0[1], s1[1]}, vo, WS_A + d * 4096 + hf * 2048 + k * 512);
            }
        }
        STAMP(22);
        lds_wait(C_H, 4);
        STAMP(23);
        issue_h();
        WAIT_VM(23);
        st_flag(rs, tag, WS_F3 + (hf * 512 + d) * FSTR);
        STAMP(24);
        preread();
    }
    v4f o[4][2];
#pragma unroll
    for (int tt = 0; tt < 4; ++tt) { o[tt][0] = (v4f){0, 0, 0, 0}; o[tt][1] = (v4f){0, 0, 0, 0}; }
    {
        const unsigned slotB = (kq < 2 ? kq * 11 + 7 : 14 + kq * 4) * 4096;
        unsigned wb[4];
#pragma unroll
        for (int tt = 0; tt < 4; ++tt) wb[tt] = (wda ^ (tt << 5)) + slotB;
#pragma unroll
        for (int i = 0; i < 11; ++i) {
            if (i < 10 || kq != 3) {
#pragma unroll
                for (int k = 0; k < 8; ++k) {
                    const int tt = k >> 1, n = (k ^ (k >> 1)) & 1;
                    o[tt][n] = MFMA16(fa[i][tt], __builtin_bit_cast(v8bf, fb[i][n]), o[tt][n]);
                    if (i >= 1 && i < 5 && (k & 1)) fa[6 + i][tt] = cat(ldstr(wb[tt] + (i - 1) * 4096), ldstr(wb[tt] + (i - 1) * 4096 + 512));
                    SB();
                }
            }
        }
    }
    STAMP(25);
    unsigned ra;
    if (role == 0) {
        poll(rs, r16 * FSTR, WS_F3 + ((im >> 4) * 512 + jd * 64 + 16 * lw) * FSTR, tag);
        const unsigned va = (lane >> 3) * 4096 + (lane & 7) * 16, sa = WS_A + (jd * 64 + 16 * lw) * 4096 + im * 128;
        v4u a0 = __builtin_amdgcn_raw_buffer_load_b128(rsb, va, sa, 16), a1 = __builtin_amdgcn_raw_buffer_load_b128(rsb, va, sa + 32768, 16);
        lds_wait(C_DONE, 4);
        ra = S_RG + lw * 8192 + lane * 16;
#pragma unroll
        for (int k = 0; k < 8; ++k) stsf4(ra + k * 1024, o[k >> 1][k & 1]);
        const unsigned ai = S_AI + (16 * lw + (lane >> 3)) * 128 + (((lane & 7) ^ (((lane >> 4) & 1) << 1)) << 4);
        stsu4(ai, a0); stsu4((ai ^ 64) + 1024, a1);
    } else {
        lds_wait(C_P0 + (kq & 1) * 4, 4);
        ra = slotA + mt * 8192 + lane * 16;
#pragma unroll
        for (int k = 0; k < 8; ++k) stsf4(ra + k * 1024, o[k >> 1][k & 1]);
    }
    LGKM0();
    STAMP(26);
    BARX();
    STAMP(27);
    {
        const unsigned pa = (lw >> 1) * 8192 + ((2 * role + (g >> 1)) * 2 + (lw & 1)) * 1024 + (g & 1) * 512 + r16 * 16;
        v4f s0 = ldsf4(S_RG + pa) + ldsf4(S_RG + 16384 + pa) + (ldsf4(pa) + ldsf4(11 * 4096 + pa));
        v4f s1 = ldsf4(S_RG + pa + 256) + ldsf4(S_RG + 16384 + pa + 256) + (ldsf4(pa + 256) + ldsf4(11 * 4096 + pa + 256));
        const unsigned aa = S_AI + role * 4096 + (wda ^ (lw << 5));
        v4u av = __builtin_bit_cast(v4u, cat(ldstr(aa), ldstr(aa + 512)));
        const float og = rbf(1.f - gate);
        unsigned bb[4] = {pk2(s0[0], s0[1]), pk2(s0[2], s0[3]), pk2(s1[0], s1[1]), pk2(s1[2], s1[3])};
        v4u ov;
#pragma unroll
        for (int k = 0; k < 4; ++k) {
            unsigned ga = pk2(gate * bflo(av[k]), gate * bfhi(av[k])), gb = pk2(og * bflo(bb[k]), og * bfhi(bb[k]));
            ov[k] = pk2(bflo(ga) + bflo(gb), bfhi(ga) + bfhi(gb));
        }
        *(v4u*)(A.out + (size_t)(im * 64 + 16 * lw + r16) * D_ + jd * 64 + 32 * role + 8 * g) = ov;
    }
    STAMP(28);
    STAMP_FLUSH(A.ws);
}

__global__ void k_probe(const unsigned* st) {
    __shared__ int a0[1024], a1[1024];
    const unsigned ref = st[0];
    for (int i = threadIdx.x; i < 1024; i += blockDim.x) { int e = ((i >> 2) * 8 + PROLE * 4 + (i & 3)) * 64; a0[i] = (int)(st[e + PK0] - ref); a1[i] = (int)(st[e + PK1] - ref); }
    __syncthreads();
    for (int s = 512; s >= 1; s >>= 1) {
        for (int i = threadIdx.x; i < s; i += blockDim.x) {
            a0[i] = PM0 == 0 ? min(a0[i], a0[i + s]) : PM0 == 1 ? max(a0[i], a0[i + s]) : a0[i] + a0[i + s];
            a1[i] = PM1 == 0 ? min(a1[i], a1[i + s]) : PM1 == 1 ? max(a1[i], a1[i + s]) : a1[i] + a1[i + s];
        }
        __syncthreads();
    }
    if (threadIdx.x == 0) {
        long long v0 = PM0 == 2 ? a0[0] : a0[0] * 1024ll, v1 = PM1 == 2 ? a1[0] : a1[0] * 1024ll;
        long long T = (v1 - v0) * PMULT / 1024;
        if (T < 0) T = 0; if (T > 100000) T = 100000;
        unsigned long long t0 = __builtin_amdgcn_s_memrealtime();
        while ((long long)(__builtin_amdgcn_s_memrealtime() - t0) < T) __builtin_amdgcn_s_sleep(1);
    }
}
extern "C" void kernel_launch(void* const* d_in, const int* in_sizes, int n_in,
                              void* d_out, int out_size, void* d_ws, size_t ws_size,
                              hipStream_t stream) {
    Args a;
    a.x = (const u16*)d_in[0]; a.Wg = (const u16*)d_in[10]; a.Wu = (const u16*)d_in[11]; a.Wd = (const u16*)d_in[12];
    a.Are = (const u16*)d_in[1]; a.Aim = (const u16*)d_in[2]; a.Bre = (const u16*)d_in[3]; a.Bim = (const u16*)d_in[4];
    a.Cre = (const u16*)d_in[5]; a.Cim = (const u16*)d_in[6]; a.logdt = (const u16*)d_in[7]; a.Dskip = (const u16*)d_in[8];
    a.gw = (const u16*)d_in[13]; a.ws = (char*)d_ws; a.out = (u16*)d_out;
    (void)hipFuncSetAttribute((const void*)k_fused, hipFuncAttributeMaxDynamicSharedMemorySize, LDS_TOTAL);
    k_fused<<<256, 512, LDS_TOTAL, stream>>>(a);
    k_probe<<<1,256,0,stream>>>((const unsigned*)((char*)d_ws + WS_STAMP));
}
```

```cpp
#include <hip/hip_runtime.h>
#include <stdint.h>

struct Srd { const void* p; unsigned bytes, fmt; };
struct Args { Srd x, wg, wu, ws, wd, out; const void* q[9]; };

__global__ __launch_bounds__(512, 2) void k_fused(Args) {
    unsigned t = threadIdx.x;
    asm volatile(
        ".Lstart_%=:\n"
        "s_load_dwordx16 s[20:35], %0, 64\n"
        "s_load_dwordx8 s[36:43], %0, 128\n"
        "s_load_dwordx16 s[4:19], %0, 0\n"
        "s_load_dwordx2 s[44:45], %0, 160\n"
        "s_mov_b32 s3, %1\n"
        "v_readfirstlane_b32 s75, %2\n"
        "s_getpc_b64 s[76:77]\n"
        ".Lpc_%=:\n"
        "v_lshlrev_b32 v27, 7, %2\n"
        "v_cmp_gt_u32 vcc, .Lend_%=-.Lpc_%=-4, v27\n"
        "s_and_b64 exec, vcc, exec\n"
        "s_cbranch_scc0 .Lnt_%=\n"
        "global_load_dword v250, v27, s[76:77]\n"
        ".Lnt_%=:\n"
        "s_mov_b64 exec, -1\n"
        "v_and_b32 v0, 63, %2\n"
        "s_bfe_u32 s46, s75, 0x20006\n"
        "v_lshlrev_b32 v3, 4, v0\n"
        "s_lshr_b32 s47, s75, 8\n"
        "v_and_b32 v26, 31, v0\n"
        "s_lshl2_add_u32 s80, s3, s46\n"
        "v_mov_b32 v27, 0\n"
        "s_lshl_b32 s80, s80, 7\n"
        "v_lshl_add_u32 v26, v26, 2, s80\n"
        "s_bfe_u32 s54, s3, 0x30003\n"
        "v_lshrrev_b32 v2, 4, v0\n"
        "s_and_b32 s75, s3, 1\n"
        "v_and_b32 v24, 7, v0\n"
        "s_lshl3_add_u32 s48, s75, s54\n"
        "v_xor_b32 v24, v24, v2\n"
        "s_bfe_u32 s49, s3, 0x20001\n"
        "v_lshrrev_b32 v25, 3, v0\n"
        "s_lshl2_add_u32 s56, s48, s46\n"
        "v_lshlrev_b32 v24, 4, v24\n"
        "s_lshl_b32 s56, s56, 15\n"
        "v_lshl_or_b32 v120, v25, 10, v24\n"
        "s_lshr_b32 s50, s3, 6\n"
        "v_xor_b32 v24, 64, v120\n"
        "s_lshl2_add_u32 s51, s49, s50\n"
        "v_add_u32 v121, 7168, v24\n"
        "s_lshr_b32 s58, s51, 1\n"
        "v_mov_b32 v34, 0x01000c0c\n"
        "s_lshl_b32 s58, s58, 7\n"
        "s_lshl_b32 s75, s47, 14\n"
        "s_add_u32 s75, s75, s58\n"
        "s_add_u32 s75, s75, s56\n"
        "s_lshl_b32 s59, s46, 12\n"
        "s_lshl_b32 s78, s47, 11\n"
        "s_add_u32 s78, s78, s59\n"
        "s_lshl_b32 s81, s3, 2\n"
        "s_add_u32 m0, s78, 81920\n"
        "s_cmp_eq_u32 s47, 0\n"
        "s_waitcnt lgkmcnt(0)\n"
        "v_lshl_add_u64 v[36:37], v[26:27], 0, s[28:29]\n"
        "s_mov_b32 exec_lo, 0\n"
        "v_lshl_add_u64 v[36:37], v[26:27], 0, s[30:31]\n"
        "v_mov_b32 v34, 0x03020c0c\n"
        "s_mov_b32 exec_lo, -1\n"
        "global_load_dword v28, v[36:37], off\n"
        "s_load_dword s80, s[42:43], s81\n"
        "s_cbranch_scc1 .Lnp_%=\n"
        "v_lshl_add_u64 v[36:37], v[26:27], 0, s[32:33]\n"
        "v_lshl_add_u64 v[38:39], v[26:27], 0, s[36:37]\n"
        "s_mov_b32 exec_lo, 0\n"
        "v_lshl_add_u64 v[36:37], v[26:27], 0, s[34:35]\n"
        "v_lshl_add_u64 v[38:39], v[26:27], 0, s[38:39]\n"
        "s_mov_b32 exec_lo, -1\n"
        "global_load_dword v30, v[36:37], off\n"
        "global_load_dword v32, v[38:39], off\n"
        "s_load_dword s81, s[44:45], s81\n"
        ".Lnp_%=:\n"
        "buffer_load_dwordx4 v120, s[4:7], s75 offen lds\n"
        "buffer_load_dwordx4 v121, s[4:7], s75 offen offset:1024 lds\n"
        "s_lshr_b32 s82, s46, 1\n"
        "v_lshrrev_b32 v25, 2, v0\n"
        "s_lshl_b32 s76, s82, 5\n"
        "v_mul_u32_u24 v25, 43, v25\n"
        "s_movk_i32 s77, 2560\n"
        "v_lshrrev_b32 v124, 7, v25\n"
        "s_mul_i32 s52, s49, 11\n"
        "v_add_u32 v24, 688, v25\n"
        "s_mul_i32 s75, s50, 3\n"
        "v_add_u32 v25, 1376, v25\n"
        "s_add_u32 s52, s52, s75\n"
        "v_lshrrev_b32 v125, 7, v24\n"
        "s_cmp_eq_u32 s51, 15\n"
        "v_lshrrev_b32 v126, 7, v25\n"
        "s_subb_u32 s52, s52, 0\n"
        "v_mad_u32_u24 v124, v124, s77, v3\n"
        "s_and_b32 s83, s46, 1\n"
        "v_and_b32 v24, 32, v0\n"
        "s_mul_i32 s57, s83, 44032\n"
        "v_xor_b32 v24, v24, v3\n"
        "s_lshl_b32 s75, s52, 6\n"
        "v_mad_u32_u24 v125, v125, s77, v24\n"
        "s_add_u32 s57, s57, s75\n"
        "v_xor_b32 v24, 32, v3\n"
        "s_lshl2_add_u32 s60, s82, s83\n"
        "v_mad_u32_u24 v126, v126, s77, v24\n"
        "s_mul_i32 s60, s60, 3072\n"
        "s_and_b32 s75, s51, 14\n"
        "s_add_u32 s75, s75, s47\n"
        "s_mul_i32 s75, s75, 88064\n"
        "s_add_u32 s75, s75, s57\n"
        "s_mul_i32 s78, s47, 6144\n"
        "s_add_u32 s78, s78, s60\n"
        "s_add_u32 m0, s78, 98304\n"
        "s_mov_b64 s[96:97], s[8:9]\n"
        "s_cmp_lg_u32 s82, 0\n"
        "s_cselect_b64 s[8:9], s[12:13], s[8:9]\n"
        "buffer_load_dwordx4 v124, s[8:11], s75 offen lds\n"
        "s_load_dwordx2 s[62:63], s[16:17], 0\n"
        "buffer_load_dwordx4 v125, s[8:11], s75 offen offset:1024 lds\n"
        "buffer_load_dwordx4 v126, s[8:11], s75 offen offset:2048 lds\n"
        "v_and_b32 v1, 15, v0\n"
        "v_bfe_u32 v24, v0, 2, 2\n"
        "v_and_b32 v25, 3, v0\n"
        "v_mov_b32 v52, 0x3fb8aa3b\n"
        "v_mov_b32 v53, 0x3e22f983\n"
        "s_cmp_eq_u32 s47, 0\n"
        "s_cbranch_scc1 .Lgemm_%=\n"
        "s_setprio 1\n"
        "v_add_u32 v122, 14336, v120\n"
        "v_add_u32 v123, 14336, v121\n"
        "v_lshl_add_u32 v26, v2, 3, v24\n"
        "v_lshrrev_b32 v27, 1, v26\n"
        "s_lshl_b32 s75, s46, 1\n"
        "v_lshrrev_b32 v156, 1, v25\n"
        "v_add_u32 v156, s75, v156\n"
        "v_xor_b32 v27, v27, v156\n"
        "v_and_b32 v27, 7, v27\n"
        "v_lshlrev_b32 v27, 4, v27\n"
        "v_lshl_add_u32 v26, v26, 7, v27\n"
        "v_and_b32 v27, 1, v25\n"
        "v_lshl_add_u32 v26, v27, 3, v26\n"
        "s_and_b32 s76, s51, 1\n"
        "s_lshl_b32 s75, s76, 13\n"
        "s_add_u32 s75, s75, 81920\n"
        "v_add_u32 v142, s75, v26\n"
        "v_xor_b32 v143, 32, v142\n"
        "v_lshrrev_b32 v26, 3, v0\n"
        "v_mul_u32_u24 v26, 8320, v26\n"
        "v_and_b32 v27, 0x70, v3\n"
        "v_add_u32 v144, v26, v27\n"
        "v_and_b32 v27, 0x3c, v0\n"
        "v_lshl_add_u32 v139, v25, 6, v27\n"
        "s_lshl1_add_u32 s76, s48, s76\n"
        "s_lshr_b32 s75, s51, 1\n"
        "s_lshl2_add_u32 s75, s75, s46\n"
        "s_mul_i32 s53, s75, 66560\n"
        "s_lshl_b32 s77, s76, 8\n"
        "s_add_u32 s53, s53, s77\n"
        "s_add_u32 s53, s53, 36864\n"
        "s_lshl_b32 s75, s75, 8\n"
        "s_lshl3_add_u32 s48, s76, s75\n"
        "s_add_u32 s48, s48, 4096\n"
        "v_pk_mov_b32 v[152:153], 0, 0\n"
        "v_pk_mov_b32 v[154:155], 0, 0\n"
        "v_pk_mov_b32 v[112:113], 0, 0\n"
        "v_pk_mov_b32 v[114:115], 0, 0\n"
        "v_mul_u32_u24 v26, 544, v1\n"
        "v_lshl_add_u32 v26, v2, 4, v26\n"
        "s_mul_i32 s75, s82, 11424\n"
        "s_add_u32 s76, s75, 57344\n"
        "v_add_u32 v140, s76, v26\n"
        "s_lshl_b32 s76, s83, 8\n"
        "s_add_u32 s96, s75, s76\n"
        "s_mul_i32 s75, s46, 8704\n"
        "v_add_u32 v141, s75, v26\n"
        "s_lshl_b32 s75, s83, 4\n"
        "v_add_u32 v26, s75, v1\n"
        "v_max_u32 v27, 1, v2\n"
        "v_sub_u32 v27, 6, v27\n"
        "v_lshlrev_b32 v27, v27, v26\n"
        "v_lshlrev_b32 v156, 4, v2\n"
        "v_lshlrev_b32 v157, 9, v2\n"
        "v_cmp_gt_u32 vcc, 2, v2\n"
        "s_lshl1_add_u32 s75, s82, 35968\n"
        "s_nop 0\n"
        "v_cndmask_b32 v156, v157, v156, vcc\n"
        "v_add3_u32 v136, v27, v156, s75\n"
        "v_cmp_eq_u32 vcc, 3, v2\n"
        "v_mov_b32 v156, 256\n"
        "v_lshl_add_u32 v157, v26, 2, v157\n"
        "v_cndmask_b32 v156, 8, v156, vcc\n"
        "v_add_u32 v137, v136, v156\n"
        "v_add_u32 v138, s75, v157\n"
        "s_cmp_eq_u32 s50, 3\n"
        "s_cselect_b32 s75, 2130706432, 0\n"
        "s_cmp_eq_u32 s51, 14\n"
        "s_cselect_b32 s75, 2130706432, s75\n"
        "s_mov_b32 s76, 0x00f00f00\n"
        "s_mov_b32 s77, 0x0f00f00f\n"
        "s_mov_b64 exec, s[76:77]\n"
        "v_or_b32 v124, s75, v124\n"
        "s_lshr_b64 exec, exec, 4\n"
        "v_or_b32 v125, s75, v125\n"
        "s_mov_b32 exec_lo, 0x0f00f00f\n"
        "s_mov_b32 exec_hi, 0xf00f00f0\n"
        "v_or_b32 v126, s75, v126\n"
        "s_mov_b64 exec, -1\n"
        "s_waitcnt lgkmcnt(0)\n"
        "s_lshl_b32 s75, s82, 4\n"
        "s_lshr_b32 s80, s80, s75\n"
        "s_lshl_b32 s80, s80, 16\n"
        "v_mul_f32 v54, s80, v52\n"
        "v_exp_f32 v54, v54\n"
        "s_add_u32 s62, s62, 1\n"
        "s_addc_u32 s63, s63, 0\n"
        "v_cvt_pk_bf16_f32 v54, v54, v54\n"
        "v_lshlrev_b32 v54, 16, v54\n"
        "s_waitcnt vmcnt(5)\n"
        "v_mov_b32 v29, v28\n"
        "v_mov_b32 v31, v30\n"
        "v_mov_b32 v33, v32\n"
        "v_permlane32_swap_b32 v28, v29\n"
        "v_permlane32_swap_b32 v30, v31\n"
        "v_permlane32_swap_b32 v32, v33\n"
        "v_perm_b32 v28, v28, v28, v34\n"
        "v_perm_b32 v29, v29, v29, v34\n"
        "v_perm_b32 v30, v30, v30, v34\n"
        "v_perm_b32 v31, v31, v31, v34\n"
        "v_perm_b32 v32, v32, v32, v34\n"
        "v_perm_b32 v33, v33, v33, v34\n"
        "v_pk_mul_f32 v[28:29], v[28:29], v[54:55] op_sel_hi:[1,0]\n"
        "v_pk_mul_f32 v[28:29], v[28:29], v[52:53]\n"
        "s_lshr_b32 s81, s81, s75\n"
        "s_lshl_b32 s81, s81, 16\n"
        "v_mul_f32 v52, 0x43800000, v28\n"
        "v_mul_f32 v53, 0x43800000, v29\n"
        "v_fract_f32 v29, v29\n"
        "v_fract_f32 v53, v53\n"
        "v_exp_f32 v28, v28\n"
        "s_cmp_eq_u32 s83, 0\n"
        "v_cos_f32 v34, v29\n"
        "s_cselect_b32 s81, s81, 0\n"
        "v_sin_f32 v35, v29\n"
        "v_exp_f32 v52, v52\n"
        "v_cos_f32 v38, v53\n"
        "v_sin_f32 v39, v53\n"
        "v_pk_mul_f32 v[34:35], v[34:35], v[28:29] op_sel_hi:[1,0]\n"
        "v_writelane_b32 v112, s81, 0\n"
        "v_pk_mul_f32 v[38:39], v[38:39], v[52:53] op_sel_hi:[1,0]\n"
        "v_pk_mul_f32 v[30:31], v[30:31], v[54:55] op_sel_hi:[1,0]\n"
        "v_pk_mul_f32 v[48:49], v[32:33], v[30:31] op_sel:[1,1] op_sel_hi:[1,0] neg_lo:[1,0]\n"
        "v_pk_fma_f32 v[42:43], v[32:33], v[30:31], v[48:49] op_sel_hi:[0,1,1]\n"
        "v_pk_mul_f32 v[50:51], v[42:43], v[38:39] op_sel:[1,1] op_sel_hi:[1,0] neg_lo:[1,0]\n"
        "v_cvt_pk_bf16_f32 v88, v42, -v43\n"
        "v_pk_fma_f32 v[44:45], v[42:43], v[38:39], v[50:51] op_sel_hi:[0,1,1]\n"
        "v_pk_mul_f32 v[48:49], v[42:43], v[34:35] op_sel:[1,1] op_sel_hi:[1,0] neg_lo:[1,0]\n"
        "v_cvt_pk_bf16_f32 v96, v44, -v45\n"
        "v_pk_fma_f32 v[30:31], v[42:43], v[34:35], v[48:49] op_sel_hi:[0,1,1]\n"
        "v_pk_mul_f32 v[50:51], v[44:45], v[38:39] op_sel:[1,1] op_sel_hi:[1,0] neg_lo:[1,0]\n"
        "v_cvt_pk_bf16_f32 v89, v30, -v31\n"
        "v_pk_fma_f32 v[44:45], v[44:45], v[38:39], v[50:51] op_sel_hi:[0,1,1]\n"
        "v_pk_mul_f32 v[48:49], v[30:31], v[34:35] op_sel:[1,1] op_sel_hi:[1,0] neg_lo:[1,0]\n"
        "v_cvt_pk_bf16_f32 v100, v44, -v45\n"
        "v_pk_fma_f32 v[42:43], v[30:31], v[34:35], v[48:49] op_sel_hi:[0,1,1]\n"
        "s_waitcnt vmcnt(3)\n"
        "s_barrier\n"
        "ds_read_b64_tr_b16 v[184:185], v142 offset:0\n"
        "ds_read_b64_tr_b16 v[186:187], v143 offset:512\n"
        "ds_read_b64_tr_b16 v[188:189], v142 offset:4096\n"
        "ds_read_b64_tr_b16 v[190:191], v143 offset:4608\n"
        "v_pk_mul_f32 v[50:51], v[44:45], v[38:39] op_sel:[1,1] op_sel_hi:[1,0] neg_lo:[1,0]\n"
        "v_cvt_pk_bf16_f32 v90, v42, -v43\n"
        "v_pk_fma_f32 v[44:45], v[44:45], v[38:39], v[50:51] op_sel_hi:[0,1,1]\n"
        "v_pk_mul_f32 v[48:49], v[42:43], v[34:35] op_sel:[1,1] op_sel_hi:[1,0] neg_lo:[1,0]\n"
        "v_cvt_pk_bf16_f32 v102, v44, -v45\n"
        "v_pk_fma_f32 v[30:31], v[42:43], v[34:35], v[48:49] op_sel_hi:[0,1,1]\n"
        "v_pk_mul_f32 v[50:51], v[44:45], v[38:39] op_sel:[1,1] op_sel_hi:[1,0] neg_lo:[1,0]\n"
        "v_pk_mul_f32 v[48:49], v[42:43], v[38:39] op_sel:[1,1] op_sel_hi:[1,0] neg_lo:[1,0]\n"
        "s_waitcnt lgkmcnt(0)\n"
        "ds_bpermute_b32 v184, v139, v184\n"
        "ds_bpermute_b32 v185, v139, v185\n"
        "ds_bpermute_b32 v186, v139, v186\n"
        "ds_bpermute_b32 v187, v139, v187\n"
        "ds_bpermute_b32 v188, v139, v188\n"
        "ds_bpermute_b32 v189, v139, v189\n"
        "ds_bpermute_b32 v190, v139, v190\n"
        "ds_bpermute_b32 v191, v139, v191\n"
        "v_pk_fma_f32 v[44:45], v[44:45], v[38:39], v[50:51] op_sel_hi:[0,1,1]\n"
        "v_pk_fma_f32 v[40:41], v[42:43], v[38:39], v[48:49] op_sel_hi:[0,1,1]\n"
        "v_cvt_pk_bf16_f32 v104, v44, -v45\n"
        "v_cvt_pk_bf16_f32 v97, v40, -v41\n"
        "v_pk_mul_f32 v[50:51], v[44:45], v[38:39] op_sel:[1,1] op_sel_hi:[1,0] neg_lo:[1,0]\n"
        "v_cvt_pk_bf16_f32 v91, v30, -v31\n"
        "v_pk_fma_f32 v[44:45], v[44:45], v[38:39], v[50:51] op_sel_hi:[0,1,1]\n"
        "v_pk_mul_f32 v[48:49], v[30:31], v[34:35] op_sel:[1,1] op_sel_hi:[1,0] neg_lo:[1,0]\n"
        "s_waitcnt lgkmcnt(0)\n"
        "buffer_store_dwordx4 v[184:187], v144, s[16:19], s53 offen offset:0 sc1\n"
        "buffer_store_dwordx4 v[188:191], v144, s[16:19], s53 offen offset:128 sc1\n"
        "v_cvt_pk_bf16_f32 v105, v44, -v45\n"
        "v_pk_fma_f32 v[42:43], v[30:31], v[34:35], v[48:49] op_sel_hi:[0,1,1]\n"
        "v_pk_mul_f32 v[50:51], v[44:45], v[38:39] op_sel:[1,1] op_sel_hi:[1,0] neg_lo:[1,0]\n"
        "v_cvt_pk_bf16_f32 v92, v42, -v43\n"
        "s_add_u32 m0, s59, 122880\n"
        "s_add_u32 s58, s58, 128\n"
        "s_and_b32 s58, s58, 1023\n"
        "s_add_u32 s12, s56, s58\n"
        "buffer_load_dwordx4 v120, s[4:7], s12 offen offset:0 lds\n"
        "s_mul_i32 s13, s58, 1376\n"
        "v_pk_fma_f32 v[44:45], v[44:45], v[38:39], v[50:51] op_sel_hi:[0,1,1]\n"
        "v_pk_mul_f32 v[48:49], v[42:43], v[34:35] op_sel:[1,1] op_sel_hi:[1,0] neg_lo:[1,0]\n"
        "v_cvt_pk_bf16_f32 v106, v44, -v45\n"
        "v_pk_fma_f32 v[30:31], v[42:43], v[34:35], v[48:49] op_sel_hi:[0,1,1]\n"
        "v_pk_mul_f32 v[50:51], v[44:45], v[38:39] op_sel:[1,1] op_sel_hi:[1,0] neg_lo:[1,0]\n"
        "buffer_load_dwordx4 v121, s[4:7], s12 offen offset:1024 lds\n"
        "s_add_u32 s13, s13, s57\n"
        "v_pk_mul_f32 v[48:49], v[42:43], v[38:39] op_sel:[1,1] op_sel_hi:[1,0] neg_lo:[1,0]\n"
        "v_pk_fma_f32 v[44:45], v[44:45], v[38:39], v[50:51] op_sel_hi:[0,1,1]\n"
        "v_pk_fma_f32 v[46:47], v[42:43], v[38:39], v[48:49] op_sel_hi:[0,1,1]\n"
        "v_cvt_pk_bf16_f32 v107, v44, -v45\n"
        "v_cvt_pk_bf16_f32 v98, v46, -v47\n"
        "buffer_load_dwordx4 v122, s[4:7], s12 offen offset:2048 lds\n"
        "s_add_u32 s14, s13, 88064\n"
        "v_pk_mul_f32 v[50:51], v[44:45], v[38:39] op_sel:[1,1] op_sel_hi:[1,0] neg_lo:[1,0]\n"
        "v_cvt_pk_bf16_f32 v93, v30, -v31\n"
        "v_pk_fma_f32 v[44:45], v[44:45], v[38:39], v[50:51] op_sel_hi:[0,1,1]\n"
        "v_pk_mul_f32 v[48:49], v[30:31], v[34:35] op_sel:[1,1] op_sel_hi:[1,0] neg_lo:[1,0]\n"
        "v_cvt_pk_bf16_f32 v108, v44, -v45\n"
        "buffer_load_dwordx4 v123, s[4:7], s12 offen offset:3072 lds\n"
        "v_pk_fma_f32 v[42:43], v[30:31], v[34:35], v[48:49] op_sel_hi:[0,1,1]\n"
        "v_pk_mul_f32 v[50:51], v[46:47], v[38:39] op_sel:[1,1] op_sel_hi:[1,0] neg_lo:[1,0]\n"
        "v_cvt_pk_bf16_f32 v94, v42, -v43\n"
        "v_pk_fma_f32 v[46:47], v[46:47], v[38:39], v[50:51] op_sel_hi:[0,1,1]\n"
        "v_pk_mul_f32 v[48:49], v[42:43], v[34:35] op_sel:[1,1] op_sel_hi:[1,0] neg_lo:[1,0]\n"
        "s_add_u32 m0, s60, 139264\n"
        "s_nop 0\n"
        "buffer_load_dwordx4 v124, s[8:11], s13 offen offset:0 lds\n"
        "v_cvt_pk_bf16_f32 v101, v46, -v47\n"
        "v_pk_fma_f32 v[30:31], v[42:43], v[34:35], v[48:49] op_sel_hi:[0,1,1]\n"
        "v_pk_mul_f32 v[50:51], v[46:47], v[38:39] op_sel:[1,1] op_sel_hi:[1,0] neg_lo:[1,0]\n"
        "v_pk_mul_f32 v[48:49], v[42:43], v[38:39] op_sel:[1,1] op_sel_hi:[1,0] neg_lo:[1,0]\n"
        "v_pk_fma_f32 v[46:47], v[46:47], v[38:39], v[50:51] op_sel_hi:[0,1,1]\n"
        "buffer_load_dwordx4 v125, s[8:11], s13 offen offset:1024 lds\n"
        "v_pk_fma_f32 v[40:41], v[42:43], v[38:39], v[48:49] op_sel_hi:[0,1,1]\n"
        "v_cvt_pk_bf16_f32 v103, v46, -v47\n"
        "v_cvt_pk_bf16_f32 v99, v40, -v41\n"
        "v_cvt_pk_bf16_f32 v95, v30, -v31\n"
        "s_waitcnt vmcnt(8)\n"
        "s_add_u32 m0, s96, 32768\n"
        "s_nop 0\n"
        "ds_write_addtid_b32 v88 offset:24576\n"
        "ds_write_addtid_b32 v89 offset:25120\n"
        "ds_write_addtid_b32 v90 offset:25664\n"
        "ds_write_addtid_b32 v91 offset:26208\n"
        "ds_write_addtid_b32 v92 offset:26752\n"
        "ds_write_addtid_b32 v93 offset:27296\n"
        "ds_write_addtid_b32 v94 offset:27840\n"
        "ds_write_addtid_b32 v95 offset:28384\n"
        "ds_write_addtid_b32 v96 offset:28928\n"
        "ds_write_addtid_b32 v97 offset:29472\n"
        "ds_write_addtid_b32 v98 offset:30016\n"
        "ds_write_addtid_b32 v99 offset:30560\n"
        "ds_write_addtid_b32 v100 offset:31104\n"
        "ds_write_addtid_b32 v101 offset:31648\n"
        "ds_write_addtid_b32 v102 offset:32192\n"
        "ds_write_addtid_b32 v103 offset:32736\n"
        "ds_write_addtid_b32 v104 offset:33280\n"
        "ds_write_addtid_b32 v105 offset:33824\n"
        "ds_write_addtid_b32 v106 offset:34368\n"
        "ds_write_addtid_b32 v107 offset:34912\n"
        "ds_write_addtid_b32 v108 offset:35456\n"
        "v_lshlrev_b32 v26, 2, v1\n"
        "ds_write_b32 v26, v152 offset:0\n"
        "ds_write_b32 v26, v152 offset:35904\n"
        "s_setprio 0\n"
        "s_branch .Lssm_%=\n"
        ".Lgemm_%=:\n"
        "s_lshl4_add_u32 s75, s83, s51\n"
        "s_cmp_eq_u32 s75, 3\n"
        "s_cselect_b32 s56, 1, 0\n"
        "s_mov_b32 m0, 34880\n"
        "s_cbranch_scc0 .Lng_1_%=\n"
        "v_add_u32 v26, 128, v3\n"
        "v_and_b32 v26, 1023, v26\n"
        "global_load_lds_dwordx4 v26, s[40:41]\n"
        ".Lng_1_%=:\n"
        "s_cmp_ge_u32 s46, 2\n"
        "s_cbranch_scc1 .Ltw_%=\n"
        "s_lshr_b32 s75, s3, 3\n"
        "v_lshrrev_b32 v27, 3, v0\n"
        "s_lshl1_add_u32 s75, s75, s83\n"
        "v_mul_u32_u24 v27, 2752, v27\n"
        "s_mul_i32 s75, s75, 22016\n"
        "v_and_b32 v26, 7, v0\n"
        "s_mul_i32 s76, s49, 704\n"
        "v_lshlrev_b32 v26, 7, v26\n"
        "s_add_u32 s75, s75, s76\n"
        "v_min_u32 v26, 700, v26\n"
        "s_bitcmp1_b32 s3, 0\n"
        "v_add_u32 v27, v27, v26\n"
        "s_cselect_b64 s[12:13], s[12:13], s[96:97]\n"
        "buffer_load_dword v248, v27, s[12:15], s75 offen\n"
        "s_branch .Ltd_%=\n"
        ".Ltw_%=:\n"
        "s_cmp_eq_u32 s46, 3\n"
        "s_cbranch_scc1 .Ltt_%=\n"
        "s_mul_i32 s75, s3, 5504\n"
        "v_min_u32 v27, 42, v0\n"
        "v_lshlrev_b32 v27, 7, v27\n"
        "buffer_load_dword v248, v27, s[20:23], s75 offen\n"
        "s_branch .Ltd_%=\n"
        ".Ltt_%=:\n"
        "s_lshl_b32 s75, s3, 9\n"
        "s_add_u32 s75, s75, 12128256\n"
        "s_min_u32 s76, s3, 191\n"
        "s_lshl_b32 s76, s76, 7\n"
        "s_add_u32 s76, s76, 4096\n"
        "v_mov_b32 v27, s76\n"
        "s_mov_b64 exec, 15\n"
        "v_lshl_add_u32 v27, v0, 7, s75\n"
        "s_mov_b64 exec, -1\n"
        "buffer_load_dword v248, v27, s[16:19], 0 offen sc1\n"
        ".Ltd_%=:\n"
        "s_lshl_b32 s75, s82, 13\n"
        "v_lshrrev_b32 v26, 1, v1\n"
        "v_mov_b32 v36, v2\n"
        "v_xor_b32 v26, v26, v2\n"
        "v_lshlrev_b32 v26, 4, v26\n"
        "v_lshl_add_u32 v26, v1, 7, v26\n"
        "v_add_u32 v152, s75, v26\n"
        "v_xor_b32 v153, 64, v152\n"
        "v_lshrrev_b32 v26, 1, v2\n"
        "v_mul_u32_u24 v26, 3072, v26\n"
        "v_and_b32 v27, 1, v2\n"
        "v_lshl_add_u32 v37, v27, 3, v24\n"
        "v_mul_u32_u24 v37, 192, v37\n"
        "v_add_u32 v26, v37, v26\n"
        "v_xor_b32 v27, s83, v27\n"
        "v_lshl_add_u32 v26, v27, 5, v26\n"
        "v_lshl_add_u32 v26, v25, 3, v26\n"
        "v_add_u32 v154, 16384, v26\n"
        "v_lshlrev_b32 v26, 4, v36\n"
        "v_add_u32 v162, 34880, v26\n"
        "v_mov_b32 v18, 0xbdd2d3e8\n"
        "v_mov_b32 v20, 0xc0135761\n"
        "v_mov_b32 v19, v18\n"
        "v_mov_b32 v21, v20\n"
        "v_mov_b32 v163, v24\n"
        "v_mov_b32 v164, v25\n"
        "v_mul_f32 v56, 0x41000000, v52\n"
        "v_mul_f32 v57, 0x41000000, v53\n"
        "v_mov_b32 v44, 1.0\n"
        "v_mov_b32 v45, 0\n"
        "s_cmp_lg_u32 s50, 3\n"
        "s_cselect_b64 vcc, -1, 0\n"
        "s_cmp_lg_u32 s51, 14\n"
        "s_cselect_b64 vcc, vcc, 0\n"
        "s_mul_i32 s75, s82, 17408\n"
        "s_lshl_b32 s76, s83, 8\n"
        "s_add_u32 m0, s75, s76\n"
        "v_add_u32 v157, 81920, v152\n"
        "v_add_u32 v158, 81920, v153\n"
        "v_add_u32 v159, 81920, v154\n"
        "s_lshl1_add_u32 s48, s48, s82\n"
        "s_mul_i32 s59, s48, 178176\n"
        "s_lshl1_add_u32 s77, s52, s83\n"
        "s_lshl_b32 s77, s77, 10\n"
        "s_add_u32 s59, s59, s77\n"
        "s_add_u32 s59, s59, 6426624\n"
        "s_mov_b32 s81, 122880\n"
        "s_mov_b32 s60, 6\n"
        "s_waitcnt lgkmcnt(0)\n"
        "s_lshl_b32 s75, s82, 4\n"
        "s_lshr_b32 s80, s80, s75\n"
        "s_lshl_b32 s80, s80, 16\n"
        "v_mul_f32 v54, s80, v52\n"
        "v_exp_f32 v54, v54\n"
        "s_add_u32 s62, s62, 1\n"
        "s_addc_u32 s63, s63, 0\n"
        "v_cvt_pk_bf16_f32 v54, v54, v54\n"
        "v_lshlrev_b32 v54, 16, v54\n"
        "s_waitcnt vmcnt(6)\n"
        "v_mov_b32 v29, v28\n"
        "s_nop 1\n"
        "v_permlane32_swap_b32 v28, v29\n"
        "v_perm_b32 v28, v28, v28, v34\n"
        "v_perm_b32 v29, v29, v29, v34\n"
        "v_pk_mul_f32 v[28:29], v[28:29], v[54:55] op_sel_hi:[1,0]\n"
        "v_pk_mul_f32 v[28:29], v[28:29], v[56:57]\n"
        "v_fract_f32 v29, v29\n"
        "v_exp_f32 v28, v28\n"
        "v_cos_f32 v34, v29\n"
        "v_sin_f32 v35, v29\n"
        "s_nop 0\n"
        "v_pk_mul_f32 v[46:47], v[34:35], v[28:29] op_sel_hi:[1,0]\n"
        "v_pk_mul_f32 v[34:35], v[34:35], v[28:29] op_sel_hi:[1,0]\n"
        "v_pk_mul_f32 v[48:49], v[34:35], v[46:47] op_sel:[1,1] op_sel_hi:[1,0] neg_lo:[1,0]\n"
        "v_pk_fma_f32 v[40:41], v[34:35], v[46:47], v[48:49] op_sel_hi:[0,1,1]\n"
        "v_pk_mul_f32 v[48:49], v[44:45], v[40:41] op_sel:[1,1] op_sel_hi:[1,0] neg_lo:[1,0]\n"
        "v_pk_mul_f32 v[50:51], v[46:47], v[40:41] op_sel:[1,1] op_sel_hi:[1,0] neg_lo:[1,0]\n"
        "v_cvt_pk_bf16_f32 v208, v44, v45\n"
        "v_cvt_pk_bf16_f32 v209, v46, v47\n"
        "v_pk_fma_f32 v[44:45], v[44:45], v[40:41], v[48:49] op_sel_hi:[0,1,1]\n"
        "v_pk_fma_f32 v[46:47], v[46:47], v[40:41], v[50:51] op_sel_hi:[0,1,1]\n"
        "v_pk_mul_f32 v[48:49], v[44:45], v[40:41] op_sel:[1,1] op_sel_hi:[1,0] neg_lo:[1,0]\n"
        "v_pk_mul_f32 v[50:51], v[46:47], v[40:41] op_sel:[1,1] op_sel_hi:[1,0] neg_lo:[1,0]\n"
        "v_cvt_pk_bf16_f32 v210, v44, v45\n"
        "v_cvt_pk_bf16_f32 v211, v46, v47\n"
        "v_pk_fma_f32 v[44:45], v[44:45], v[40:41], v[48:49] op_sel_hi:[0,1,1]\n"
        "v_pk_fma_f32 v[46:47], v[46:47], v[40:41], v[50:51] op_sel_hi:[0,1,1]\n"
        "v_pk_mul_f32 v[48:49], v[44:45], v[40:41] op_sel:[1,1] op_sel_hi:[1,0] neg_lo:[1,0]\n"
        "v_pk_mul_f32 v[50:51], v[46:47], v[40:41] op_sel:[1,1] op_sel_hi:[1,0] neg_lo:[1,0]\n"
        "v_cvt_pk_bf16_f32 v212, v44, v45\n"
        "v_cvt_pk_bf16_f32 v213, v46, v47\n"
        "v_pk_fma_f32 v[44:45], v[44:45], v[40:41], v[48:49] op_sel_hi:[0,1,1]\n"
        "v_pk_fma_f32 v[46:47], v[46:47], v[40:41], v[50:51] op_sel_hi:[0,1,1]\n"
        "v_pk_mul_f32 v[48:49], v[44:45], v[40:41] op_sel:[1,1] op_sel_hi:[1,0] neg_lo:[1,0]\n"
        "v_pk_mul_f32 v[50:51], v[46:47], v[40:41] op_sel:[1,1] op_sel_hi:[1,0] neg_lo:[1,0]\n"
        "v_cvt_pk_bf16_f32 v214, v44, v45\n"
        "v_cvt_pk_bf16_f32 v215, v46, v47\n"
        "v_pk_fma_f32 v[44:45], v[44:45], v[40:41], v[48:49] op_sel_hi:[0,1,1]\n"
        "v_pk_fma_f32 v[46:47], v[46:47], v[40:41], v[50:51] op_sel_hi:[0,1,1]\n"
        "v_pk_mul_f32 v[48:49], v[44:45], v[40:41] op_sel:[1,1] op_sel_hi:[1,0] neg_lo:[1,0]\n"
        "v_pk_mul_f32 v[50:51], v[46:47], v[40:41] op_sel:[1,1] op_sel_hi:[1,0] neg_lo:[1,0]\n"
        "v_cvt_pk_bf16_f32 v216, v44, v45\n"
        "v_cvt_pk_bf16_f32 v217, v46, v47\n"
        "v_pk_fma_f32 v[44:45], v[44:45], v[40:41], v[48:49] op_sel_hi:[0,1,1]\n"
        "v_pk_fma_f32 v[46:47], v[46:47], v[40:41], v[50:51] op_sel_hi:[0,1,1]\n"
        "v_pk_mul_f32 v[48:49], v[44:45], v[40:41] op_sel:[1,1] op_sel_hi:[1,0] neg_lo:[1,0]\n"
        "v_pk_mul_f32 v[50:51], v[46:47], v[40:41] op_sel:[1,1] op_sel_hi:[1,0] neg_lo:[1,0]\n"
        "v_cvt_pk_bf16_f32 v218, v44, v45\n"
        "v_cvt_pk_bf16_f32 v219, v46, v47\n"
        "v_pk_fma_f32 v[44:45], v[44:45], v[40:41], v[48:49] op_sel_hi:[0,1,1]\n"
        "v_pk_fma_f32 v[46:47], v[46:47], v[40:41], v[50:51] op_sel_hi:[0,1,1]\n"
        "s_waitcnt vmcnt(4)\n"
        "s_barrier\n"
        "v_pk_mul_f32 v[48:49], v[44:45], v[40:41] op_sel:[1,1] op_sel_hi:[1,0] neg_lo:[1,0]\n"
        "v_pk_mul_f32 v[50:51], v[46:47], v[40:41] op_sel:[1,1] op_sel_hi:[1,0] neg_lo:[1,0]\n"
        "v_cvt_pk_bf16_f32 v220, v44, v45\n"
        "v_cvt_pk_bf16_f32 v221, v46, v47\n"
        "v_pk_fma_f32 v[44:45], v[44:45], v[40:41], v[48:49] op_sel_hi:[0,1,1]\n"
        "v_pk_fma_f32 v[46:47], v[46:47], v[40:41], v[50:51] op_sel_hi:[0,1,1]\n"
        "v_pk_mul_f32 v[48:49], v[44:45], v[40:41] op_sel:[1,1] op_sel_hi:[1,0] neg_lo:[1,0]\n"
        "v_pk_mul_f32 v[50:51], v[46:47], v[40:41] op_sel:[1,1] op_sel_hi:[1,0] neg_lo:[1,0]\n"
        "v_cvt_pk_bf16_f32 v222, v44, v45\n"
        "v_cvt_pk_bf16_f32 v223, v46, v47\n"
        "v_pk_fma_f32 v[44:45], v[44:45], v[40:41], v[48:49] op_sel_hi:[0,1,1]\n"
        "v_pk_fma_f32 v[46:47], v[46:47], v[40:41], v[50:51] op_sel_hi:[0,1,1]\n"
        "v_pk_mul_f32 v[48:49], v[44:45], v[40:41] op_sel:[1,1] op_sel_hi:[1,0] neg_lo:[1,0]\n"
        "v_pk_mul_f32 v[50:51], v[46:47], v[40:41] op_sel:[1,1] op_sel_hi:[1,0] neg_lo:[1,0]\n"
        "v_cvt_pk_bf16_f32 v224, v44, v45\n"
        "v_cvt_pk_bf16_f32 v225, v46, v47\n"
        "v_pk_fma_f32 v[44:45], v[44:45], v[40:41], v[48:49] op_sel_hi:[0,1,1]\n"
        "v_pk_fma_f32 v[46:47], v[46:47], v[40:41], v[50:51] op_sel_hi:[0,1,1]\n"
        "v_pk_mul_f32 v[48:49], v[44:45], v[40:41] op_sel:[1,1] op_sel_hi:[1,0] neg_lo:[1,0]\n"
        "v_pk_mul_f32 v[50:51], v[46:47], v[40:41] op_sel:[1,1] op_sel_hi:[1,0] neg_lo:[1,0]\n"
        "v_cvt_pk_bf16_f32 v226, v44, v45\n"
        "v_cvt_pk_bf16_f32 v227, v46, v47\n"
        "v_pk_fma_f32 v[44:45], v[44:45], v[40:41], v[48:49] op_sel_hi:[0,1,1]\n"
        "v_pk_fma_f32 v[46:47], v[46:47], v[40:41], v[50:51] op_sel_hi:[0,1,1]\n"
        "v_pk_mul_f32 v[48:49], v[44:45], v[40:41] op_sel:[1,1] op_sel_hi:[1,0] neg_lo:[1,0]\n"
        "v_pk_mul_f32 v[50:51], v[46:47], v[40:41] op_sel:[1,1] op_sel_hi:[1,0] neg_lo:[1,0]\n"
        "v_cvt_pk_bf16_f32 v228, v44, v45\n"
        "v_cvt_pk_bf16_f32 v229, v46, v47\n"
        "v_pk_fma_f32 v[44:45], v[44:45], v[40:41], v[48:49] op_sel_hi:[0,1,1]\n"
        "v_pk_fma_f32 v[46:47], v[46:47], v[40:41], v[50:51] op_sel_hi:[0,1,1]\n"
        "v_pk_mul_f32 v[48:49], v[44:45], v[40:41] op_sel:[1,1] op_sel_hi:[1,0] neg_lo:[1,0]\n"
        "v_pk_mul_f32 v[50:51], v[46:47], v[40:41] op_sel:[1,1] op_sel_hi:[1,0] neg_lo:[1,0]\n"
        "v_cvt_pk_bf16_f32 v230, v44, v45\n"
        "v_cvt_pk_bf16_f32 v231, v46, v47\n"
        "v_pk_fma_f32 v[44:45], v[44:45], v[40:41], v[48:49] op_sel_hi:[0,1,1]\n"
        "v_pk_fma_f32 v[46:47], v[46:47], v[40:41], v[50:51] op_sel_hi:[0,1,1]\n"
        "v_pk_mul_f32 v[48:49], v[44:45], v[40:41] op_sel:[1,1] op_sel_hi:[1,0] neg_lo:[1,0]\n"
        "v_pk_mul_f32 v[50:51], v[46:47], v[40:41] op_sel:[1,1] op_sel_hi:[1,0] neg_lo:[1,0]\n"
        "v_cvt_pk_bf16_f32 v232, v44, v45\n"
        "v_cvt_pk_bf16_f32 v233, v46, v47\n"
        "v_pk_fma_f32 v[44:45], v[44:45], v[40:41], v[48:49] op_sel_hi:[0,1,1]\n"
        "v_pk_fma_f32 v[46:47], v[46:47], v[40:41], v[50:51] op_sel_hi:[0,1,1]\n"
        "v_pk_mul_f32 v[48:49], v[44:45], v[40:41] op_sel:[1,1] op_sel_hi:[1,0] neg_lo:[1,0]\n"
        "v_pk_mul_f32 v[50:51], v[46:47], v[40:41] op_sel:[1,1] op_sel_hi:[1,0] neg_lo:[1,0]\n"
        "v_cvt_pk_bf16_f32 v234, v44, v45\n"
        "v_cvt_pk_bf16_f32 v235, v46, v47\n"
        "v_pk_fma_f32 v[44:45], v[44:45], v[40:41], v[48:49] op_sel_hi:[0,1,1]\n"
        "v_pk_fma_f32 v[46:47], v[46:47], v[40:41], v[50:51] op_sel_hi:[0,1,1]\n"
        "v_pk_mul_f32 v[48:49], v[44:45], v[40:41] op_sel:[1,1] op_sel_hi:[1,0] neg_lo:[1,0]\n"
        "v_pk_mul_f32 v[50:51], v[46:47], v[40:41] op_sel:[1,1] op_sel_hi:[1,0] neg_lo:[1,0]\n"
        "v_cvt_pk_bf16_f32 v236, v44, v45\n"
        "v_cvt_pk_bf16_f32 v237, v46, v47\n"
        "v_pk_fma_f32 v[44:45], v[44:45], v[40:41], v[48:49] op_sel_hi:[0,1,1]\n"
        "v_pk_fma_f32 v[46:47], v[46:47], v[40:41], v[50:51] op_sel_hi:[0,1,1]\n"
        "v_cvt_pk_bf16_f32 v238, v44, v45\n"
        "v_cvt_pk_bf16_f32 v239, v46, v47\n"
        "s_waitcnt vmcnt(1)\n"
        "ds_write_addtid_b32 v208 offset:64\n"
        "ds_write_addtid_b32 v209 offset:608\n"
        "ds_write_addtid_b32 v210 offset:1152\n"
        "ds_write_addtid_b32 v211 offset:1696\n"
        "ds_write_addtid_b32 v212 offset:2240\n"
        "ds_write_addtid_b32 v213 offset:2784\n"
        "ds_write_addtid_b32 v214 offset:3328\n"
        "ds_write_addtid_b32 v215 offset:3872\n"
        "ds_write_addtid_b32 v216 offset:4416\n"
        "ds_write_addtid_b32 v217 offset:4960\n"
        "ds_write_addtid_b32 v218 offset:5504\n"
        "ds_write_addtid_b32 v219 offset:6048\n"
        "ds_write_addtid_b32 v220 offset:6592\n"
        "ds_write_addtid_b32 v221 offset:7136\n"
        "ds_write_addtid_b32 v222 offset:7680\n"
        "ds_write_addtid_b32 v223 offset:8224\n"
        "ds_write_addtid_b32 v224 offset:8768\n"
        "ds_write_addtid_b32 v225 offset:9312\n"
        "ds_write_addtid_b32 v226 offset:9856\n"
        "ds_write_addtid_b32 v227 offset:10400\n"
        "ds_write_addtid_b32 v228 offset:10944\n"
        "ds_write_addtid_b32 v229 offset:11488\n"
        "ds_write_addtid_b32 v230 offset:12032\n"
        "ds_write_addtid_b32 v231 offset:12576\n"
        "ds_write_addtid_b32 v232 offset:13120\n"
        "ds_write_addtid_b32 v233 offset:13664\n"
        "ds_write_addtid_b32 v234 offset:14208\n"
        "ds_write_addtid_b32 v235 offset:14752\n"
        "ds_write_addtid_b32 v236 offset:15296\n"
        "ds_write_addtid_b32 v237 offset:15840\n"
        "ds_write_addtid_b32 v238 offset:16384\n"
        "ds_write_addtid_b32 v239 offset:16928\n"
        "s_setprio 1\n"
        "s_waitcnt lgkmcnt(0)\n"
        "s_barrier\n"
        "ds_read_b128 v[168:171], v157 offset:0\n"
        "ds_read_b128 v[172:175], v157 offset:2048\n"
        "ds_read_b128 v[176:179], v157 offset:4096\n"
        "ds_read_b128 v[180:183], v157 offset:6144\n"
        "ds_read_b64_tr_b16 v[184:185], v159 offset:0\n"
        "ds_read_b64_tr_b16 v[186:187], v159 offset:768\n"
        "ds_read_b64_tr_b16 v[188:189], v159 offset:12288\n"
        "ds_read_b64_tr_b16 v[190:191], v159 offset:13056\n"
        "ds_read_b64_tr_b16 v[192:193], v159 offset:64\n"
        "ds_read_b64_tr_b16 v[194:195], v159 offset:832\n"
        "ds_read_b64_tr_b16 v[196:197], v159 offset:12352\n"
        "ds_read_b64_tr_b16 v[198:199], v159 offset:13120\n"
        "ds_read_b64_tr_b16 v[200:201], v159 offset:128\n"
        "ds_read_b64_tr_b16 v[202:203], v159 offset:896\n"
        "ds_read_b64_tr_b16 v[204:205], v159 offset:12416\n"
        "ds_read_b64_tr_b16 v[206:207], v159 offset:13184\n"
        "s_cmp_eq_u32 s56, 0\n"
        "s_cbranch_scc1 .Lmid_%=\n"
        "ds_read_b128 v[200:203], v162\n"
        "v_add_u32 v162, 64, v162\n"
        ".Lmid_%=:\n"
        "s_waitcnt lgkmcnt(0)\n"
        "v_mfma_f32_16x16x32_bf16 v[56:59], v[184:187], v[168:171], 0\n"
        "ds_read_b128 v[208:211], v158 offset:0\n"
        "v_mfma_f32_16x16x32_bf16 v[60:63], v[184:187], v[172:175], 0\n"
        "ds_read_b128 v[212:215], v158 offset:2048\n"
        "v_mfma_f32_16x16x32_bf16 v[64:67], v[184:187], v[176:179], 0\n"
        "ds_read_b128 v[216:219], v158 offset:4096\n"
        "v_mfma_f32_16x16x32_bf16 v[68:71], v[184:187], v[180:183], 0\n"
        "ds_read_b128 v[220:223], v158 offset:6144\n"
        "v_mfma_f32_16x16x32_bf16 v[84:87], v[188:191], v[180:183], 0\n"
        "ds_read_b64_tr_b16 v[224:225], v159 offset:6144\n"
        "v_mfma_f32_16x16x32_bf16 v[80:83], v[188:191], v[176:179], 0\n"
        "ds_read_b64_tr_b16 v[226:227], v159 offset:6912\n"
        "v_mfma_f32_16x16x32_bf16 v[76:79], v[188:191], v[172:175], 0\n"
        "ds_read_b64_tr_b16 v[228:229], v159 offset:18432\n"
        "v_mfma_f32_16x16x32_bf16 v[72:75], v[188:191], v[168:171], 0\n"
        "ds_read_b64_tr_b16 v[230:231], v159 offset:19200\n"
        "v_mfma_f32_16x16x32_bf16 v[88:91], v[192:195], v[168:171], 0\n"
        "ds_read_b64_tr_b16 v[232:233], v159 offset:6208\n"
        "v_mfma_f32_16x16x32_bf16 v[92:95], v[192:195], v[172:175], 0\n"
        "ds_read_b64_tr_b16 v[234:235], v159 offset:6976\n"
        "s_barrier\n"
        "v_mfma_f32_16x16x32_bf16 v[96:99], v[192:195], v[176:179], 0\n"
        "ds_read_b64_tr_b16 v[236:237], v159 offset:18496\n"
        "v_mfma_f32_16x16x32_bf16 v[100:103], v[192:195], v[180:183], 0\n"
        "ds_read_b64_tr_b16 v[238:239], v159 offset:19264\n"
        "v_mfma_f32_16x16x32_bf16 v[116:119], v[196:199], v[180:183], 0\n"
        "ds_read_b64_tr_b16 v[240:241], v159 offset:6272\n"
        "v_mfma_f32_16x16x32_bf16 v[112:115], v[196:199], v[176:179], 0\n"
        "ds_read_b64_tr_b16 v[242:243], v159 offset:7040\n"
        "v_mfma_f32_16x16x32_bf16 v[108:111], v[196:199], v[172:175], 0\n"
        "ds_read_b64_tr_b16 v[244:245], v159 offset:18560\n"
        "v_mfma_f32_16x16x32_bf16 v[104:107], v[196:199], v[168:171], 0\n"
        "ds_read_b64_tr_b16 v[246:247], v159 offset:19328\n"
        "v_add_u32 v157, s81, v152\n"
        "v_add_u32 v158, s81, v153\n"
        "v_add_u32 v159, s81, v154\n"
        "s_cbranch_vccz .Lalt_2_%=\n"
        ".Lfull_4_%=:\n"
        "v_mfma_f32_16x16x32_bf16 v[120:123], v[200:203], v[168:171], 0\n"
        "v_mfma_f32_16x16x32_bf16 v[124:127], v[200:203], v[172:175], 0\n"
        "v_mfma_f32_16x16x32_bf16 v[128:131], v[200:203], v[176:179], 0\n"
        "v_mfma_f32_16x16x32_bf16 v[132:135], v[200:203], v[180:183], 0\n"
        "v_mfma_f32_16x16x32_bf16 v[148:151], v[204:207], v[180:183], 0\n"
        "v_mfma_f32_16x16x32_bf16 v[144:147], v[204:207], v[176:179], 0\n"
        "v_mfma_f32_16x16x32_bf16 v[140:143], v[204:207], v[172:175], 0\n"
        "v_mfma_f32_16x16x32_bf16 v[136:139], v[204:207], v[168:171], 0\n"
        ".Lback_3_%=:\n"
        "s_waitcnt lgkmcnt(0)\n"
        "s_barrier\n"
        ".Lloop_%=:\n"
        "v_mfma_f32_16x16x32_bf16 v[56:59], v[224:227], v[208:211], v[56:59]\n"
        "ds_read_b128 v[168:171], v157 offset:0\n"
        "v_mfma_f32_16x16x32_bf16 v[60:63], v[224:227], v[212:215], v[60:63]\n"
        "ds_read_b128 v[172:175], v157 offset:2048\n"
        "v_mfma_f32_16x16x32_bf16 v[64:67], v[224:227], v[216:219], v[64:67]\n"
        "ds_read_b128 v[176:179], v157 offset:4096\n"
        "v_mfma_f32_16x16x32_bf16 v[68:71], v[224:227], v[220:223], v[68:71]\n"
        "ds_read_b128 v[180:183], v157 offset:6144\n"
        "v_mfma_f32_16x16x32_bf16 v[84:87], v[228:231], v[220:223], v[84:87]\n"
        "ds_read_b64_tr_b16 v[184:185], v159 offset:0\n"
        "v_mfma_f32_16x16x32_bf16 v[80:83], v[228:231], v[216:219], v[80:83]\n"
        "ds_read_b64_tr_b16 v[186:187], v159 offset:768\n"
        "v_mfma_f32_16x16x32_bf16 v[76:79], v[228:231], v[212:215], v[76:79]\n"
        "ds_read_b64_tr_b16 v[188:189], v159 offset:12288\n"
        "v_mfma_f32_16x16x32_bf16 v[72:75], v[228:231], v[208:211], v[72:75]\n"
        "ds_read_b64_tr_b16 v[190:191], v159 offset:13056\n"
        "v_mfma_f32_16x16x32_bf16 v[88:91], v[232:235], v[208:211], v[88:91]\n"
        "ds_read_b64_tr_b16 v[192:193], v159 offset:64\n"
        "v_mfma_f32_16x16x32_bf16 v[92:95], v[232:235], v[212:215], v[92:95]\n"
        "ds_read_b64_tr_b16 v[194:195], v159 offset:832\n"
        "v_mfma_f32_16x16x32_bf16 v[96:99], v[232:235], v[216:219], v[96:99]\n"
        "ds_read_b64_tr_b16 v[196:197], v159 offset:12352\n"
        "v_mfma_f32_16x16x32_bf16 v[100:103], v[232:235], v[220:223], v[100:103]\n"
        "ds_read_b64_tr_b16 v[198:199], v159 offset:13120\n"
        "v_mfma_f32_16x16x32_bf16 v[116:119], v[236:239], v[220:223], v[116:119]\n"
        "ds_read_b64_tr_b16 v[200:201], v159 offset:128\n"
        "v_mfma_f32_16x16x32_bf16 v[112:115], v[236:239], v[216:219], v[112:115]\n"
        "ds_read_b64_tr_b16 v[202:203], v159 offset:896\n"
        "v_mfma_f32_16x16x32_bf16 v[108:111], v[236:239], v[212:215], v[108:111]\n"
        "ds_read_b64_tr_b16 v[204:205], v159 offset:12416\n"
        "v_mfma_f32_16x16x32_bf16 v[104:107], v[236:239], v[208:211], v[104:107]\n"
        "ds_read_b64_tr_b16 v[206:207], v159 offset:13184\n"
        "s_add_u32 s81, s81, 40960\n"
        "s_cmp_eq_u32 s81, 163840\n"
        "s_cselect_b32 s81, 40960, s81\n"
        "s_cbranch_vccz .Lalt_5_%=\n"
        ".Lfull_7_%=:\n"
        "v_mfma_f32_16x16x32_bf16 v[120:123], v[240:243], v[208:211], v[120:123]\n"
        "v_mfma_f32_16x16x32_bf16 v[124:127], v[240:243], v[212:215], v[124:127]\n"
        "v_mfma_f32_16x16x32_bf16 v[128:131], v[240:243], v[216:219], v[128:131]\n"
        "v_mfma_f32_16x16x32_bf16 v[132:135], v[240:243], v[220:223], v[132:135]\n"
        "v_mfma_f32_16x16x32_bf16 v[148:151], v[244:247], v[220:223], v[148:151]\n"
        "v_mfma_f32_16x16x32_bf16 v[144:147], v[244:247], v[216:219], v[144:147]\n"
        "v_mfma_f32_16x16x32_bf16 v[140:143], v[244:247], v[212:215], v[140:143]\n"
        "v_mfma_f32_16x16x32_bf16 v[136:139], v[244:247], v[208:211], v[136:139]\n"
        ".Lback_6_%=:\n"
        "s_waitcnt lgkmcnt(0)\n"
        "v_mfma_f32_16x16x32_bf16 v[56:59], v[184:187], v[168:171], v[56:59]\n"
        "ds_read_b128 v[208:211], v158 offset:0\n"
        "v_mfma_f32_16x16x32_bf16 v[60:63], v[184:187], v[172:175], v[60:63]\n"
        "ds_read_b128 v[212:215], v158 offset:2048\n"
        "v_mfma_f32_16x16x32_bf16 v[64:67], v[184:187], v[176:179], v[64:67]\n"
        "ds_read_b128 v[216:219], v158 offset:4096\n"
        "v_mfma_f32_16x16x32_bf16 v[68:71], v[184:187], v[180:183], v[68:71]\n"
        "ds_read_b128 v[220:223], v158 offset:6144\n"
        "v_mfma_f32_16x16x32_bf16 v[84:87], v[188:191], v[180:183], v[84:87]\n"
        "ds_read_b64_tr_b16 v[224:225], v159 offset:6144\n"
        "v_mfma_f32_16x16x32_bf16 v[80:83], v[188:191], v[176:179], v[80:83]\n"
        "ds_read_b64_tr_b16 v[226:227], v159 offset:6912\n"
        "v_mfma_f32_16x16x32_bf16 v[76:79], v[188:191], v[172:175], v[76:79]\n"
        "ds_read_b64_tr_b16 v[228:229], v159 offset:18432\n"
        "v_mfma_f32_16x16x32_bf16 v[72:75], v[188:191], v[168:171], v[72:75]\n"
        "ds_read_b64_tr_b16 v[230:231], v159 offset:19200\n"
        "v_mfma_f32_16x16x32_bf16 v[88:91], v[192:195], v[168:171], v[88:91]\n"
        "ds_read_b64_tr_b16 v[232:233], v159 offset:6208\n"
        "v_mfma_f32_16x16x32_bf16 v[92:95], v[192:195], v[172:175], v[92:95]\n"
        "ds_read_b64_tr_b16 v[234:235], v159 offset:6976\n"
        "v_mfma_f32_16x16x32_bf16 v[96:99], v[192:195], v[176:179], v[96:99]\n"
        "ds_read_b64_tr_b16 v[236:237], v159 offset:18496\n"
        "v_mfma_f32_16x16x32_bf16 v[100:103], v[192:195], v[180:183], v[100:103]\n"
        "ds_read_b64_tr_b16 v[238:239], v159 offset:19264\n"
        "v_mfma_f32_16x16x32_bf16 v[116:119], v[196:199], v[180:183], v[116:119]\n"
        "ds_read_b64_tr_b16 v[240:241], v159 offset:6272\n"
        "v_mfma_f32_16x16x32_bf16 v[112:115], v[196:199], v[176:179], v[112:115]\n"
        "ds_read_b64_tr_b16 v[242:243], v159 offset:7040\n"
        "v_mfma_f32_16x16x32_bf16 v[108:111], v[196:199], v[172:175], v[108:111]\n"
        "ds_read_b64_tr_b16 v[244:245], v159 offset:18560\n"
        "v_mfma_f32_16x16x32_bf16 v[104:107], v[196:199], v[168:171], v[104:107]\n"
        "ds_read_b64_tr_b16 v[246:247], v159 offset:19328\n"
        "v_add_u32 v157, s81, v152\n"
        "v_add_u32 v158, s81, v153\n"
        "v_add_u32 v159, s81, v154\n"
        "s_cbranch_vccz .Lalt_8_%=\n"
        ".Lfull_10_%=:\n"
        "v_mfma_f32_16x16x32_bf16 v[120:123], v[200:203], v[168:171], v[120:123]\n"
        "v_mfma_f32_16x16x32_bf16 v[124:127], v[200:203], v[172:175], v[124:127]\n"
        "v_mfma_f32_16x16x32_bf16 v[128:131], v[200:203], v[176:179], v[128:131]\n"
        "v_mfma_f32_16x16x32_bf16 v[132:135], v[200:203], v[180:183], v[132:135]\n"
        "v_mfma_f32_16x16x32_bf16 v[148:151], v[204:207], v[180:183], v[148:151]\n"
        "v_mfma_f32_16x16x32_bf16 v[144:147], v[204:207], v[176:179], v[144:147]\n"
        "v_mfma_f32_16x16x32_bf16 v[140:143], v[204:207], v[172:175], v[140:143]\n"
        "v_mfma_f32_16x16x32_bf16 v[136:139], v[204:207], v[168:171], v[136:139]\n"
        ".Lback_9_%=:\n"
        "s_sub_u32 s60, s60, 1\n"
        "s_waitcnt lgkmcnt(0)\n"
        "s_barrier\n"
        "s_cbranch_scc0 .Lloop_%=\n"
        "v_mfma_f32_16x16x32_bf16 v[56:59], v[224:227], v[208:211], v[56:59]\n"
        "s_and_b32 s75, s50, 1\n"
        "v_and_b32 v26, 1, v2\n"
        "v_mfma_f32_16x16x32_bf16 v[60:63], v[224:227], v[212:215], v[60:63]\n"
        "s_mul_i32 s97, s75, 22\n"
        "s_lshl_b32 s77, s83, 1\n"
        "v_mfma_f32_16x16x32_bf16 v[64:67], v[224:227], v[216:219], v[64:67]\n"
        "v_or_b32 v26, s77, v26\n"
        "s_lshr_b32 s75, s50, 1\n"
        "v_mfma_f32_16x16x32_bf16 v[68:71], v[224:227], v[220:223], v[68:71]\n"
        "v_lshlrev_b32 v26, 5, v26\n"
        "s_mul_i32 s75, s75, 7\n"
        "v_mfma_f32_16x16x32_bf16 v[84:87], v[228:231], v[220:223], v[84:87]\n"
        "v_xor_b32 v26, v26, v3\n"
        "s_add_u32 s97, s97, s75\n"
        "v_mfma_f32_16x16x32_bf16 v[80:83], v[228:231], v[216:219], v[80:83]\n"
        "v_and_b32 v26, 0x7f, v26\n"
        "s_mul_i32 s75, s54, 0\n"
        "v_mfma_f32_16x16x32_bf16 v[76:79], v[228:231], v[212:215], v[76:79]\n"
        "v_lshrrev_b32 v27, 3, v0\n"
        "s_lshr_b32 s75, s75, 1\n"
        "v_mfma_f32_16x16x32_bf16 v[72:75], v[228:231], v[208:211], v[72:75]\n"
        "v_lshl_add_u32 v22, v27, 10, v26\n"
        "s_add_u32 s97, s97, s75\n"
        "v_mfma_f32_16x16x32_bf16 v[88:91], v[232:235], v[208:211], v[88:91]\n"
        "s_lshl2_add_u32 s80, s97, s46\n"
        "s_lshl_b32 s80, s80, 6\n"
        "v_mfma_f32_16x16x32_bf16 v[92:95], v[232:235], v[212:215], v[92:95]\n"
        "s_add_u32 s80, s80, s54\n"
        "s_lshl_b32 s80, s80, 7\n"
        "v_mfma_f32_16x16x32_bf16 v[96:99], v[232:235], v[216:219], v[96:99]\n"
        "s_lshl_b32 s55, s46, 10\n"
        "v_mov_b32 v4, 0\n"
        "v_mfma_f32_16x16x32_bf16 v[100:103], v[232:235], v[220:223], v[100:103]\n"
        "v_mov_b32 v5, 1\n"
        "v_mov_b32 v6, s62\n"
        "v_mfma_f32_16x16x32_bf16 v[116:119], v[236:239], v[220:223], v[116:119]\n"
        "v_mov_b32 v7, s63\n"
        "s_mov_b32 s61, 0\n"
        "v_mfma_f32_16x16x32_bf16 v[112:115], v[236:239], v[216:219], v[112:115]\n"
        "s_lshl1_add_u32 s76, s51, s83\n"
        "s_lshl_b32 s76, s76, 7\n"
        "v_mfma_f32_16x16x32_bf16 v[108:111], v[236:239], v[212:215], v[108:111]\n"
        "s_lshl_b32 s96, s48, 12\n"
        "s_add_u32 s96, s96, s76\n"
        "v_mfma_f32_16x16x32_bf16 v[104:107], v[236:239], v[208:211], v[104:107]\n"
        "s_add_u32 s96, s96, 12128256\n"
        "s_and_b32 s75, s3, 7\n"
        "v_and_b32 v26, 31, v0\n"
        "s_lshl2_add_u32 s53, s75, s50\n"
        "v_lshlrev_b32 v255, 7, v26\n"
        "s_lshl_b32 s32, s53, 12\n"
        "s_add_u32 s32, s32, 12128256\n"
        "s_cbranch_vccz .Lalt_11_%=\n"
        ".Lfull_13_%=:\n"
        "v_mfma_f32_16x16x32_bf16 v[120:123], v[240:243], v[208:211], v[120:123]\n"
        "v_mfma_f32_16x16x32_bf16 v[124:127], v[240:243], v[212:215], v[124:127]\n"
        "v_mfma_f32_16x16x32_bf16 v[128:131], v[240:243], v[216:219], v[128:131]\n"
        "v_mfma_f32_16x16x32_bf16 v[132:135], v[240:243], v[220:223], v[132:135]\n"
        "v_mfma_f32_16x16x32_bf16 v[148:151], v[244:247], v[220:223], v[148:151]\n"
        "v_mfma_f32_16x16x32_bf16 v[144:147], v[244:247], v[216:219], v[144:147]\n"
        "v_mfma_f32_16x16x32_bf16 v[140:143], v[244:247], v[212:215], v[140:143]\n"
        "v_mfma_f32_16x16x32_bf16 v[136:139], v[244:247], v[208:211], v[136:139]\n"
        ".Lback_12_%=:\n"
        "v_pk_mul_f32 v[24:25], v[56:57], v[56:57]\n"
        "v_pk_mul_f32 v[26:27], v[58:59], v[58:59]\n"
        "v_pk_mul_f32 v[28:29], v[60:61], v[60:61]\n"
        "v_pk_mul_f32 v[30:31], v[62:63], v[62:63]\n"
        "v_pk_fma_f32 v[24:25], v[24:25], v[18:19], v[20:21]\n"
        "v_pk_fma_f32 v[26:27], v[26:27], v[18:19], v[20:21]\n"
        "v_pk_fma_f32 v[28:29], v[28:29], v[18:19], v[20:21]\n"
        "v_pk_fma_f32 v[30:31], v[30:31], v[18:19], v[20:21]\n"
        "v_pk_mul_f32 v[24:25], v[24:25], v[56:57]\n"
        "v_pk_mul_f32 v[26:27], v[26:27], v[58:59]\n"
        "v_pk_mul_f32 v[28:29], v[28:29], v[60:61]\n"
        "v_pk_mul_f32 v[30:31], v[30:31], v[62:63]\n"
        "v_exp_f32 v24, v24\n"
        "v_exp_f32 v25, v25\n"
        "v_exp_f32 v26, v26\n"
        "v_exp_f32 v27, v27\n"
        "v_exp_f32 v28, v28\n"
        "v_exp_f32 v29, v29\n"
        "v_exp_f32 v30, v30\n"
        "v_exp_f32 v31, v31\n"
        "v_pk_add_f32 v[24:25], v[24:25], 1.0 op_sel_hi:[1,0]\n"
        "v_pk_add_f32 v[26:27], v[26:27], 1.0 op_sel_hi:[1,0]\n"
        "v_pk_add_f32 v[28:29], v[28:29], 1.0 op_sel_hi:[1,0]\n"
        "v_pk_add_f32 v[30:31], v[30:31], 1.0 op_sel_hi:[1,0]\n"
        "v_pk_mul_f32 v[72:73], v[72:73], v[56:57]\n"
        "v_pk_mul_f32 v[74:75], v[74:75], v[58:59]\n"
        "v_pk_mul_f32 v[76:77], v[76:77], v[60:61]\n"
        "v_pk_mul_f32 v[78:79], v[78:79], v[62:63]\n"
        "v_rcp_f32 v24, v24\n"
        "v_rcp_f32 v25, v25\n"
        "v_rcp_f32 v26, v26\n"
        "v_rcp_f32 v27, v27\n"
        "v_rcp_f32 v28, v28\n"
        "v_rcp_f32 v29, v29\n"
        "v_rcp_f32 v30, v30\n"
        "v_rcp_f32 v31, v31\n"
        "v_pk_mul_f32 v[72:73], v[72:73], v[24:25]\n"
        "v_pk_mul_f32 v[74:75], v[74:75], v[26:27]\n"
        "v_pk_mul_f32 v[76:77], v[76:77], v[28:29]\n"
        "v_pk_mul_f32 v[78:79], v[78:79], v[30:31]\n"
        "v_cvt_pk_bf16_f32 v32, v72, v73\n"
        "v_cvt_pk_bf16_f32 v34, v76, v77\n"
        "v_cvt_pk_bf16_f32 v33, v74, v75\n"
        "v_cvt_pk_bf16_f32 v35, v78, v79\n"
        "v_permlane16_swap_b32 v32, v34\n"
        "s_nop 0\n"
        "v_permlane16_swap_b32 v33, v35\n"
        "s_barrier\n"
        "v_pk_mul_f32 v[24:25], v[64:65], v[64:65]\n"
        "v_pk_mul_f32 v[26:27], v[66:67], v[66:67]\n"
        "v_pk_mul_f32 v[28:29], v[68:69], v[68:69]\n"
        "v_pk_mul_f32 v[30:31], v[70:71], v[70:71]\n"
        "v_pk_fma_f32 v[24:25], v[24:25], v[18:19], v[20:21]\n"
        "v_pk_fma_f32 v[26:27], v[26:27], v[18:19], v[20:21]\n"
        "v_pk_fma_f32 v[28:29], v[28:29], v[18:19], v[20:21]\n"
        "v_pk_fma_f32 v[30:31], v[30:31], v[18:19], v[20:21]\n"
        "v_pk_mul_f32 v[24:25], v[24:25], v[64:65]\n"
        "v_pk_mul_f32 v[26:27], v[26:27], v[66:67]\n"
        "v_pk_mul_f32 v[28:29], v[28:29], v[68:69]\n"
        "v_pk_mul_f32 v[30:31], v[30:31], v[70:71]\n"
        "v_exp_f32 v24, v24\n"
        "v_exp_f32 v25, v25\n"
        "v_exp_f32 v26, v26\n"
        "v_exp_f32 v27, v27\n"
        "v_exp_f32 v28, v28\n"
        "v_exp_f32 v29, v29\n"
        "v_exp_f32 v30, v30\n"
        "v_exp_f32 v31, v31\n"
        "v_pk_add_f32 v[24:25], v[24:25], 1.0 op_sel_hi:[1,0]\n"
        "v_pk_add_f32 v[26:27], v[26:27], 1.0 op_sel_hi:[1,0]\n"
        "v_pk_add_f32 v[28:29], v[28:29], 1.0 op_sel_hi:[1,0]\n"
        "v_pk_add_f32 v[30:31], v[30:31], 1.0 op_sel_hi:[1,0]\n"
        "v_pk_mul_f32 v[80:81], v[80:81], v[64:65]\n"
        "v_pk_mul_f32 v[82:83], v[82:83], v[66:67]\n"
        "v_pk_mul_f32 v[84:85], v[84:85], v[68:69]\n"
        "v_pk_mul_f32 v[86:87], v[86:87], v[70:71]\n"
        "v_rcp_f32 v24, v24\n"
        "v_rcp_f32 v25, v25\n"
        "v_rcp_f32 v26, v26\n"
        "v_rcp_f32 v27, v27\n"
        "v_rcp_f32 v28, v28\n"
        "v_rcp_f32 v29, v29\n"
        "v_rcp_f32 v30, v30\n"
        "v_rcp_f32 v31, v31\n"
        "v_pk_mul_f32 v[80:81], v[80:81], v[24:25]\n"
        "v_pk_mul_f32 v[82:83], v[82:83], v[26:27]\n"
        "v_pk_mul_f32 v[84:85], v[84:85], v[28:29]\n"
        "v_pk_mul_f32 v[86:87], v[86:87], v[30:31]\n"
        "v_cvt_pk_bf16_f32 v36, v80, v81\n"
        "v_cvt_pk_bf16_f32 v38, v84, v85\n"
        "v_cvt_pk_bf16_f32 v37, v82, v83\n"
        "v_cvt_pk_bf16_f32 v39, v86, v87\n"
        "s_add_u32 s75, s59, 89088\n"
        "v_permlane16_swap_b32 v36, v38\n"
        "v_permlane16_swap_b32 v37, v39\n"
        "buffer_store_dwordx4 v[36:39], v3, s[16:19], s75 offen sc1\n"
        "s_add_u32 s75, s59, 0\n"
        "buffer_store_dwordx4 v[32:35], v3, s[16:19], s75 offen sc1\n"
        "v_pk_mul_f32 v[24:25], v[88:89], v[88:89]\n"
        "v_pk_mul_f32 v[26:27], v[90:91], v[90:91]\n"
        "v_pk_mul_f32 v[28:29], v[92:93], v[92:93]\n"
        "v_pk_mul_f32 v[30:31], v[94:95], v[94:95]\n"
        "v_pk_fma_f32 v[24:25], v[24:25], v[18:19], v[20:21]\n"
        "v_pk_fma_f32 v[26:27], v[26:27], v[18:19], v[20:21]\n"
        "v_pk_fma_f32 v[28:29], v[28:29], v[18:19], v[20:21]\n"
        "v_pk_fma_f32 v[30:31], v[30:31], v[18:19], v[20:21]\n"
        "v_pk_mul_f32 v[24:25], v[24:25], v[88:89]\n"
        "v_pk_mul_f32 v[26:27], v[26:27], v[90:91]\n"
        "v_pk_mul_f32 v[28:29], v[28:29], v[92:93]\n"
        "v_pk_mul_f32 v[30:31], v[30:31], v[94:95]\n"
        "v_exp_f32 v24, v24\n"
        "v_exp_f32 v25, v25\n"
        "v_exp_f32 v26, v26\n"
        "v_exp_f32 v27, v27\n"
        "v_exp_f32 v28, v28\n"
        "v_exp_f32 v29, v29\n"
        "v_exp_f32 v30, v30\n"
        "v_exp_f32 v31, v31\n"
        "v_pk_add_f32 v[24:25], v[24:25], 1.0 op_sel_hi:[1,0]\n"
        "v_pk_add_f32 v[26:27], v[26:27], 1.0 op_sel_hi:[1,0]\n"
        "v_pk_add_f32 v[28:29], v[28:29], 1.0 op_sel_hi:[1,0]\n"
        "v_pk_add_f32 v[30:31], v[30:31], 1.0 op_sel_hi:[1,0]\n"
        "v_pk_mul_f32 v[104:105], v[104:105], v[88:89]\n"
        "v_pk_mul_f32 v[106:107], v[106:107], v[90:91]\n"
        "v_pk_mul_f32 v[108:109], v[108:109], v[92:93]\n"
        "v_pk_mul_f32 v[110:111], v[110:111], v[94:95]\n"
        "v_rcp_f32 v24, v24\n"
        "v_rcp_f32 v25, v25\n"
        "v_rcp_f32 v26, v26\n"
        "v_rcp_f32 v27, v27\n"
        "v_rcp_f32 v28, v28\n"
        "v_rcp_f32 v29, v29\n"
        "v_rcp_f32 v30, v30\n"
        "v_rcp_f32 v31, v31\n"
        "v_pk_mul_f32 v[104:105], v[104:105], v[24:25]\n"
        "v_pk_mul_f32 v[106:107], v[106:107], v[26:27]\n"
        "v_pk_mul_f32 v[108:109], v[108:109], v[28:29]\n"
        "v_pk_mul_f32 v[110:111], v[110:111], v[30:31]\n"
        "v_cvt_pk_bf16_f32 v40, v104, v105\n"
        "v_cvt_pk_bf16_f32 v42, v108, v109\n"
        "v_cvt_pk_bf16_f32 v41, v106, v107\n"
        "v_cvt_pk_bf16_f32 v43, v110, v111\n"
        "s_add_u32 s75, s59, 2048\n"
        "v_permlane16_swap_b32 v40, v42\n"
        "v_permlane16_swap_b32 v41, v43\n"
        "buffer_store_dwordx4 v[40:43], v3, s[16:19], s75 offen sc1\n"
        "v_pk_mul_f32 v[24:25], v[96:97], v[96:97]\n"
        "v_pk_mul_f32 v[26:27], v[98:99], v[98:99]\n"
        "v_pk_mul_f32 v[28:29], v[100:101], v[100:101]\n"
        "v_pk_mul_f32 v[30:31], v[102:103], v[102:103]\n"
        "v_pk_fma_f32 v[24:25], v[24:25], v[18:19], v[20:21]\n"
        "v_pk_fma_f32 v[26:27], v[26:27], v[18:19], v[20:21]\n"
        "v_pk_fma_f32 v[28:29], v[28:29], v[18:19], v[20:21]\n"
        "v_pk_fma_f32 v[30:31], v[30:31], v[18:19], v[20:21]\n"
        "v_pk_mul_f32 v[24:25], v[24:25], v[96:97]\n"
        "v_pk_mul_f32 v[26:27], v[26:27], v[98:99]\n"
        "v_pk_mul_f32 v[28:29], v[28:29], v[100:101]\n"
        "v_pk_mul_f32 v[30:31], v[30:31], v[102:103]\n"
        "v_exp_f32 v24, v24\n"
        "v_exp_f32 v25, v25\n"
        "v_exp_f32 v26, v26\n"
        "v_exp_f32 v27, v27\n"
        "v_exp_f32 v28, v28\n"
        "v_exp_f32 v29, v29\n"
        "v_exp_f32 v30, v30\n"
        "v_exp_f32 v31, v31\n"
        "v_pk_add_f32 v[24:25], v[24:25], 1.0 op_sel_hi:[1,0]\n"
        "v_pk_add_f32 v[26:27], v[26:27], 1.0 op_sel_hi:[1,0]\n"
        "v_pk_add_f32 v[28:29], v[28:29], 1.0 op_sel_hi:[1,0]\n"
        "v_pk_add_f32 v[30:31], v[30:31], 1.0 op_sel_hi:[1,0]\n"
        "v_pk_mul_f32 v[112:113], v[112:113], v[96:97]\n"
        "v_pk_mul_f32 v[114:115], v[114:115], v[98:99]\n"
        "v_pk_mul_f32 v[116:117], v[116:117], v[100:101]\n"
        "v_pk_mul_f32 v[118:119], v[118:119], v[102:103]\n"
        "v_rcp_f32 v24, v24\n"
        "v_rcp_f32 v25, v25\n"
        "v_rcp_f32 v26, v26\n"
        "v_rcp_f32 v27, v27\n"
        "v_rcp_f32 v28, v28\n"
        "v_rcp_f32 v29, v29\n"
        "v_rcp_f32 v30, v30\n"
        "v_rcp_f32 v31, v31\n"
        "v_pk_mul_f32 v[112:113], v[112:113], v[24:25]\n"
        "v_pk_mul_f32 v[114:115], v[114:115], v[26:27]\n"
        "v_pk_mul_f32 v[116:117], v[116:117], v[28:29]\n"
        "v_pk_mul_f32 v[118:119], v[118:119], v[30:31]\n"
        "v_cvt_pk_bf16_f32 v44, v112, v113\n"
        "v_cvt_pk_bf16_f32 v46, v116, v117\n"
        "v_cvt_pk_bf16_f32 v45, v114, v115\n"
        "v_cvt_pk_bf16_f32 v47, v118, v119\n"
        "s_add_u32 s75, s59, 91136\n"
        "v_permlane16_swap_b32 v44, v46\n"
        "v_permlane16_swap_b32 v45, v47\n"
        "buffer_store_dwordx4 v[44:47], v3, s[16:19], s75 offen sc1\n"
        "s_cbranch_vccz .Lgate_%=\n"
        "v_pk_mul_f32 v[24:25], v[120:121], v[120:121]\n"
        "v_pk_mul_f32 v[26:27], v[122:123], v[122:123]\n"
        "v_pk_mul_f32 v[28:29], v[124:125], v[124:125]\n"
        "v_pk_mul_f32 v[30:31], v[126:127], v[126:127]\n"
        "v_pk_fma_f32 v[24:25], v[24:25], v[18:19], v[20:21]\n"
        "v_pk_fma_f32 v[26:27], v[26:27], v[18:19], v[20:21]\n"
        "v_pk_fma_f32 v[28:29], v[28:29], v[18:19], v[20:21]\n"
        "v_pk_fma_f32 v[30:31], v[30:31], v[18:19], v[20:21]\n"
        "v_pk_mul_f32 v[24:25], v[24:25], v[120:121]\n"
        "v_pk_mul_f32 v[26:27], v[26:27], v[122:123]\n"
        "v_pk_mul_f32 v[28:29], v[28:29], v[124:125]\n"
        "v_pk_mul_f32 v[30:31], v[30:31], v[126:127]\n"
        "v_exp_f32 v24, v24\n"
        "v_exp_f32 v25, v25\n"
        "v_exp_f32 v26, v26\n"
        "v_exp_f32 v27, v27\n"
        "v_exp_f32 v28, v28\n"
        "v_exp_f32 v29, v29\n"
        "v_exp_f32 v30, v30\n"
        "v_exp_f32 v31, v31\n"
        "v_pk_add_f32 v[24:25], v[24:25], 1.0 op_sel_hi:[1,0]\n"
        "v_pk_add_f32 v[26:27], v[26:27], 1.0 op_sel_hi:[1,0]\n"
        "v_pk_add_f32 v[28:29], v[28:29], 1.0 op_sel_hi:[1,0]\n"
        "v_pk_add_f32 v[30:31], v[30:31], 1.0 op_sel_hi:[1,0]\n"
        "v_pk_mul_f32 v[136:137], v[136:137], v[120:121]\n"
        "v_pk_mul_f32 v[138:139], v[138:139], v[122:123]\n"
        "v_pk_mul_f32 v[140:141], v[140:141], v[124:125]\n"
        "v_pk_mul_f32 v[142:143], v[142:143], v[126:127]\n"
        "v_rcp_f32 v24, v24\n"
        "v_rcp_f32 v25, v25\n"
        "v_rcp_f32 v26, v26\n"
        "v_rcp_f32 v27, v27\n"
        "v_rcp_f32 v28, v28\n"
        "v_rcp_f32 v29, v29\n"
        "v_rcp_f32 v30, v30\n"
        "v_rcp_f32 v31, v31\n"
        "v_pk_mul_f32 v[136:137], v[136:137], v[24:25]\n"
        "v_pk_mul_f32 v[138:139], v[138:139], v[26:27]\n"
        "v_pk_mul_f32 v[140:141], v[140:141], v[28:29]\n"
        "v_pk_mul_f32 v[142:143], v[142:143], v[30:31]\n"
        "v_cvt_pk_bf16_f32 v48, v136, v137\n"
        "v_cvt_pk_bf16_f32 v50, v140, v141\n"
        "v_cvt_pk_bf16_f32 v49, v138, v139\n"
        "v_cvt_pk_bf16_f32 v51, v142, v143\n"
        "s_add_u32 s75, s59, 4096\n"
        "v_permlane16_swap_b32 v48, v50\n"
        "v_permlane16_swap_b32 v49, v51\n"
        "buffer_store_dwordx4 v[48:51], v3, s[16:19], s75 offen sc1\n"
        "v_pk_mul_f32 v[24:25], v[128:129], v[128:129]\n"
        "v_pk_mul_f32 v[26:27], v[130:131], v[130:131]\n"
        "v_pk_mul_f32 v[28:29], v[132:133], v[132:133]\n"
        "v_pk_mul_f32 v[30:31], v[134:135], v[134:135]\n"
        "v_pk_fma_f32 v[24:25], v[24:25], v[18:19], v[20:21]\n"
        "v_pk_fma_f32 v[26:27], v[26:27], v[18:19], v[20:21]\n"
        "v_pk_fma_f32 v[28:29], v[28:29], v[18:19], v[20:21]\n"
        "v_pk_fma_f32 v[30:31], v[30:31], v[18:19], v[20:21]\n"
        "v_pk_mul_f32 v[24:25], v[24:25], v[128:129]\n"
        "v_pk_mul_f32 v[26:27], v[26:27], v[130:131]\n"
        "v_pk_mul_f32 v[28:29], v[28:29], v[132:133]\n"
        "v_pk_mul_f32 v[30:31], v[30:31], v[134:135]\n"
        "v_exp_f32 v24, v24\n"
        "v_exp_f32 v25, v25\n"
        "v_exp_f32 v26, v26\n"
        "v_exp_f32 v27, v27\n"
        "v_exp_f32 v28, v28\n"
        "v_exp_f32 v29, v29\n"
        "v_exp_f32 v30, v30\n"
        "v_exp_f32 v31, v31\n"
        "v_pk_add_f32 v[24:25], v[24:25], 1.0 op_sel_hi:[1,0]\n"
        "v_pk_add_f32 v[26:27], v[26:27], 1.0 op_sel_hi:[1,0]\n"
        "v_pk_add_f32 v[28:29], v[28:29], 1.0 op_sel_hi:[1,0]\n"
        "v_pk_add_f32 v[30:31], v[30:31], 1.0 op_sel_hi:[1,0]\n"
        "v_pk_mul_f32 v[144:145], v[144:145], v[128:129]\n"
        "v_pk_mul_f32 v[146:147], v[146:147], v[130:131]\n"
        "v_pk_mul_f32 v[148:149], v[148:149], v[132:133]\n"
        "v_pk_mul_f32 v[150:151], v[150:151], v[134:135]\n"
        "v_rcp_f32 v24, v24\n"
        "v_rcp_f32 v25, v25\n"
        "v_rcp_f32 v26, v26\n"
        "v_rcp_f32 v27, v27\n"
        "v_rcp_f32 v28, v28\n"
        "v_rcp_f32 v29, v29\n"
        "v_rcp_f32 v30, v30\n"
        "v_rcp_f32 v31, v31\n"
        "v_pk_mul_f32 v[144:145], v[144:145], v[24:25]\n"
        "v_pk_mul_f32 v[146:147], v[146:147], v[26:27]\n"
        "v_pk_mul_f32 v[148:149], v[148:149], v[28:29]\n"
        "v_pk_mul_f32 v[150:151], v[150:151], v[30:31]\n"
        "v_cvt_pk_bf16_f32 v52, v144, v145\n"
        "v_cvt_pk_bf16_f32 v54, v148, v149\n"
        "v_cvt_pk_bf16_f32 v53, v146, v147\n"
        "v_cvt_pk_bf16_f32 v55, v150, v151\n"
        "s_add_u32 s75, s59, 93184\n"
        "v_permlane16_swap_b32 v52, v54\n"
        "v_permlane16_swap_b32 v53, v55\n"
        "buffer_store_dwordx4 v[52:55], v3, s[16:19], s75 offen sc1\n"
        ".Lepid_%=:\n"
        "s_barrier\n"
        "s_setprio 0\n"
        "s_add_u32 m0, s55, 45056\n"
        "s_add_u32 s80, s80, 688128\n"
        "s_sub_u32 s15, s80, 1409024\n"
        "s_min_u32 s80, s80, s15\n"
        "buffer_load_dwordx4 v22, s[20:23], s80 offen lds\n"
        "v_lshl_add_u32 v26, v2, 3, v163\n"
        "v_lshlrev_b32 v26, 7, v26\n"
        "v_lshl_add_u32 v26, v164, 3, v26\n"
        "v_bfe_u32 v27, v0, 3, 2\n"
        "v_lshl_add_u32 v26, v27, 5, v26\n"
        "s_mul_i32 s75, s82, 45056\n"
        "s_add_u32 s75, s75, 45056\n"
        "v_add_u32 v8, s75, v26\n"
        "v_xor_b32 v9, 32, v8\n"
        "v_xor_b32 v10, 64, v8\n"
        "v_xor_b32 v11, 0x60, v8\n"
        "v_lshlrev_b32 v26, 9, v2\n"
        "v_lshl_add_u32 v12, v1, 4, v26\n"
        "s_mul_i32 s75, s82, 11\n"
        "s_add_u32 s75, s75, s97\n"
        "s_add_u32 s75, s75, 21\n"
        "v_bfe_u32 v26, v0, 1, 4\n"
        "v_add_u32 v27, 1, v26\n"
        "v_bfm_b32 v27, v27, 0\n"
        "v_and_b32 v27, 0xc888, v27\n"
        "v_bcnt_u32_b32 v13, v27, s75\n"
        "v_lshrrev_b32 v27, v26, v27\n"
        "s_add_u32 m0, s55, 90112\n"
        "s_add_u32 s80, s80, 360448\n"
        "s_sub_u32 s15, s80, 1409024\n"
        "s_min_u32 s80, s80, s15\n"
        "buffer_load_dwordx4 v22, s[20:23], s80 offen lds\n"
        "v_mul_u32_u24 v26, 3, v26\n"
        "v_sub_u32 v26, v26, v13\n"
        "v_add_u32 v26, 88, v26\n"
        "v_subrev_u32 v13, 43, v26\n"
        "v_min_u32 v26, v26, v13\n"
        "v_subrev_u32 v13, 43, v26\n"
        "v_min_u32 v26, v26, v13\n"
        "v_and_b32 v13, 32, v0\n"
        "v_add_u32 v26, v26, v27\n"
        "v_lshl_add_u32 v26, v13, 1, v26\n"
        "v_cmp_ge_u32 s[34:35], 4, v26\n"
        "v_cmp_ge_u32 s[36:37], 7, v26\n"
        "v_cmp_ge_u32 s[38:39], 12, v26\n"
        "s_lshl_b32 s75, s75, 11\n"
        "s_lshl1_add_u32 s76, s53, s83\n"
        "s_mul_i32 s76, s76, 89088\n"
        "s_add_u32 s76, s76, 6426624\n"
        "s_sub_u32 s77, s75, 88064\n"
        "s_min_u32 s75, s75, s77\n"
        "s_add_u32 s64, s75, s76\n"
        "s_add_u32 s75, s75, 2048\n"
        "s_sub_u32 s77, s75, 88064\n"
        "s_add_u32 m0, s55, 49152\n"
        "s_add_u32 s80, s80, 1081344\n"
        "s_sub_u32 s15, s80, 1409024\n"
        "s_min_u32 s80, s80, s15\n"
        "buffer_load_dwordx4 v22, s[20:23], s80 offen lds\n"
        "s_min_u32 s75, s75, s77\n"
        "s_add_u32 s65, s75, s76\n"
        "s_add_u32 s75, s75, 2048\n"
        "s_sub_u32 s77, s75, 88064\n"
        "s_min_u32 s75, s75, s77\n"
        "s_add_u32 s66, s75, s76\n"
        "s_add_u32 s75, s75, 2048\n"
        "s_sub_u32 s77, s75, 88064\n"
        "s_min_u32 s75, s75, s77\n"
        "s_add_u32 s67, s75, s76\n"
        "s_add_u32 s75, s75, 2048\n"
        "s_sub_u32 s77, s75, 88064\n"
        "s_min_u32 s75, s75, s77\n"
        "s_add_u32 s68, s75, s76\n"
        "s_add_u32 s75, s75, 2048\n"
        "s_sub_u32 s77, s75, 88064\n"
        "s_min_u32 s75, s75, s77\n"
        "s_add_u32 s69, s75, s76\n"
        "s_add_u32 s75, s75, 2048\n"
        "s_sub_u32 s77, s75, 88064\n"
        "s_min_u32 s75, s75, s77\n"
        "s_add_u32 s70, s75, s76\n"
        "s_add_u32 m0, s55, 94208\n"
        "s_add_u32 s80, s80, 360448\n"
        "s_sub_u32 s15, s80, 1409024\n"
        "s_min_u32 s80, s80, s15\n"
        "buffer_load_dwordx4 v22, s[20:23], s80 offen lds\n"
        "s_add_u32 s75, s75, 2048\n"
        "s_sub_u32 s77, s75, 88064\n"
        "s_min_u32 s75, s75, s77\n"
        "s_add_u32 s71, s75, s76\n"
        "s_add_u32 s75, s75, 2048\n"
        "s_sub_u32 s77, s75, 88064\n"
        "s_min_u32 s75, s75, s77\n"
        "s_add_u32 s72, s75, s76\n"
        "s_add_u32 s75, s75, 2048\n"
        "s_sub_u32 s77, s75, 88064\n"
        "s_min_u32 s75, s75, s77\n"
        "s_add_u32 s73, s75, s76\n"
        "s_add_u32 s75, s75, 2048\n"
        "s_sub_u32 s77, s75, 88064\n"
        "s_min_u32 s75, s75, s77\n"
        "s_add_u32 s74, s75, s76\n"
        "s_lshl2_add_u32 s49, s53, s46\n"
        "s_lshl_b32 s49, s49, 7\n"
        "s_add_u32 s49, s49, 12288\n"
        "s_lshl_b32 s81, s46, 13\n"
        "s_add_u32 s81, s81, 1024\n"
        "s_waitcnt vmcnt(4)\n"
        "s_mov_b64 exec, 1\n"
        "buffer_store_dwordx2 v[6:7], off, s[16:19], s96 sc1\n"
        "s_mov_b64 exec, -1\n"
        "s_add_u32 m0, s55, 53248\n"
        "s_add_u32 s80, s80, 1081344\n"
        "s_sub_u32 s15, s80, 1409024\n"
        "s_min_u32 s80, s80, s15\n"
        "buffer_load_dwordx4 v22, s[20:23], s80 offen lds\n"
        "s_add_u32 m0, s55, 98304\n"
        "s_add_u32 s80, s80, 360448\n"
        "s_sub_u32 s15, s80, 1409024\n"
        "s_min_u32 s80, s80, s15\n"
        "buffer_load_dwordx4 v22, s[20:23], s80 offen lds\n"
        "s_add_u32 m0, s55, 57344\n"
        "s_add_u32 s80, s80, 1081344\n"
        "s_sub_u32 s15, s80, 1409024\n"
        "s_min_u32 s80, s80, s15\n"
        "buffer_load_dwordx4 v22, s[20:23], s80 offen lds\n"
        "s_add_u32 m0, s55, 102400\n"
        "s_add_u32 s80, s80, 360448\n"
        "s_sub_u32 s15, s80, 1409024\n"
        "s_min_u32 s80, s80, s15\n"
        "buffer_load_dwordx4 v22, s[20:23], s80 offen lds\n"
        "s_add_u32 m0, s55, 61440\n"
        "s_add_u32 s80, s80, 1081344\n"
        "s_sub_u32 s15, s80, 1409024\n"
        "s_min_u32 s80, s80, s15\n"
        "buffer_load_dwordx4 v22, s[20:23], s80 offen lds\n"
        "s_add_u32 m0, s55, 106496\n"
        "s_add_u32 s80, s80, 360448\n"
        "s_sub_u32 s15, s80, 1409024\n"
        "s_min_u32 s80, s80, s15\n"
        "buffer_load_dwordx4 v22, s[20:23], s80 offen lds\n"
        "s_add_u32 m0, s55, 65536\n"
        "s_add_u32 s80, s80, 1081344\n"
        "s_sub_u32 s15, s80, 1409024\n"
        "s_min_u32 s80, s80, s15\n"
        "buffer_load_dwordx4 v22, s[20:23], s80 offen lds\n"
        "s_add_u32 m0, s55, 110592\n"
        "s_add_u32 s80, s80, 360448\n"
        "s_sub_u32 s15, s80, 1409024\n"
        "s_min_u32 s80, s80, s15\n"
        "buffer_load_dwordx4 v22, s[20:23], s80 offen lds\n"
        "s_add_u32 m0, s55, 69632\n"
        "s_add_u32 s80, s80, 1081344\n"
        "s_sub_u32 s15, s80, 1409024\n"
        "s_min_u32 s80, s80, s15\n"
        "buffer_load_dwordx4 v22, s[20:23], s80 offen lds\n"
        "s_add_u32 m0, s55, 114688\n"
        "s_add_u32 s80, s80, 360448\n"
        "s_sub_u32 s15, s80, 1409024\n"
        "s_min_u32 s80, s80, s15\n"
        "buffer_load_dwordx4 v22, s[20:23], s80 offen lds\n"
        "s_add_u32 m0, s55, 147456\n"
        "s_add_u32 s80, s80, 491520\n"
        "s_sub_u32 s15, s80, 1409024\n"
        "s_min_u32 s80, s80, s15\n"
        "buffer_load_dwordx4 v22, s[20:23], s80 offen lds\n"
        "s_add_u32 m0, s55, 151552\n"
        "s_add_u32 s80, s80, 262144\n"
        "s_sub_u32 s15, s80, 1409024\n"
        "s_min_u32 s80, s80, s15\n"
        "buffer_load_dwordx4 v22, s[20:23], s80 offen lds\n"
        "s_add_u32 m0, s55, 155648\n"
        "s_add_u32 s80, s80, 32768\n"
        "s_sub_u32 s15, s80, 1409024\n"
        "s_min_u32 s80, s80, s15\n"
        "buffer_load_dwordx4 v22, s[20:23], s80 offen lds\n"
        "s_add_u32 m0, s55, 159744\n"
        "s_add_u32 s80, s80, 32768\n"
        "s_sub_u32 s15, s80, 1409024\n"
        "s_min_u32 s80, s80, s15\n"
        "buffer_load_dwordx4 v22, s[20:23], s80 offen lds\n"
        "s_mov_b64 exec, s[38:39]\n"
        "buffer_load_dwordx2 v[14:15], v255, s[16:19], s32 offen sc1\n"
        "s_mov_b64 exec, -1\n"
        "s_waitcnt vmcnt(0)\n"
        "v_cmp_ne_u64 s[42:43], s[62:63], v[14:15]\n"
        "s_mov_b64 s[44:45], 1\n"
        "s_and_b32 s33, s42, s34\n"
        "s_cbranch_scc0 .Lok_%=\n"
        "s_and_b64 exec, s[42:43], s[38:39]\n"
        "buffer_load_dwordx2 v[14:15], v255, s[16:19], s32 offen sc1\n"
        "s_waitcnt vmcnt(0)\n"
        "v_cmp_ne_u64 s[42:43], s[62:63], v[14:15]\n"
        "s_mov_b64 exec, -1\n"
        "s_mov_b64 exec, 1\n"
        "ds_add_u32 v4, v5 offset:8\n"
        "s_mov_b64 exec, -1\n"
        "s_mov_b32 s44, 0\n"
        "s_and_b32 s33, s42, s34\n"
        "s_cbranch_scc0 .Lcpd_15_%=\n"
        ".Lcp_14_%=:\n"
        "s_and_b64 exec, s[42:43], s[38:39]\n"
        "buffer_load_dwordx2 v[14:15], v255, s[16:19], s32 offen sc1\n"
        "s_waitcnt vmcnt(0)\n"
        "v_cmp_ne_u64 s[42:43], s[62:63], v[14:15]\n"
        "s_mov_b64 exec, -1\n"
        "s_and_b32 s33, s42, s34\n"
        "s_cbranch_scc0 .Lcpd_15_%=\n"
        "s_sleep 1\n"
        "s_add_u32 s61, s61, 16\n"
        "s_cmp_lt_u32 s61, 0x100000\n"
        "s_cbranch_scc1 .Lcp_14_%=\n"
        ".Lcpd_15_%=:\n"
        ".Lok_%=:\n"
        "buffer_load_dwordx4 v[192:195], v12, s[16:19], s64 offen sc1\n"
        "buffer_load_dwordx4 v[196:199], v12, s[16:19], s64 offen offset:256 sc1\n"
        "buffer_load_dwordx4 v[200:203], v12, s[16:19], s65 offen sc1\n"
        "buffer_load_dwordx4 v[204:207], v12, s[16:19], s65 offen offset:256 sc1\n"
        "buffer_load_dwordx4 v[208:211], v12, s[16:19], s66 offen sc1\n"
        "buffer_load_dwordx4 v[212:215], v12, s[16:19], s66 offen offset:256 sc1\n"
        "s_waitcnt vmcnt(7)\n"
        "s_mov_b64 exec, s[44:45]\n"
        "ds_add_u32 v4, v5 offset:8\n"
        "s_mov_b64 exec, -1\n"
        ".Llw_16_%=:\n"
        "ds_read_b32 v13, v4 offset:8\n"
        "s_waitcnt lgkmcnt(0)\n"
        "v_readfirstlane_b32 s75, v13\n"
        "s_cmp_ge_u32 s75, 4\n"
        "s_cbranch_scc1 .Llwd_17_%=\n"
        "s_add_u32 s61, s61, 1\n"
        "s_cmp_lt_u32 s61, 0x100000\n"
        "s_cbranch_scc1 .Llw_16_%=\n"
        ".Llwd_17_%=:\n"
        "ds_read_b64_tr_b16 v[56:57], v8 offset:0\n"
        "ds_read_b64_tr_b16 v[58:59], v8 offset:512\n"
        "ds_read_b64_tr_b16 v[60:61], v9 offset:0\n"
        "ds_read_b64_tr_b16 v[62:63], v9 offset:512\n"
        "ds_read_b64_tr_b16 v[64:65], v10 offset:0\n"
        "ds_read_b64_tr_b16 v[66:67], v10 offset:512\n"
        "ds_read_b64_tr_b16 v[68:69], v11 offset:0\n"
        "ds_read_b64_tr_b16 v[70:71], v11 offset:512\n"
        "ds_read_b64_tr_b16 v[72:73], v8 offset:4096\n"
        "ds_read_b64_tr_b16 v[74:75], v8 offset:4608\n"
        "ds_read_b64_tr_b16 v[76:77], v9 offset:4096\n"
        "ds_read_b64_tr_b16 v[78:79], v9 offset:4608\n"
        "ds_read_b64_tr_b16 v[80:81], v10 offset:4096\n"
        "ds_read_b64_tr_b16 v[82:83], v10 offset:4608\n"
        "ds_read_b64_tr_b16 v[84:85], v11 offset:4096\n"
        "ds_read_b64_tr_b16 v[86:87], v11 offset:4608\n"
        "ds_read_b64_tr_b16 v[88:89], v8 offset:8192\n"
        "ds_read_b64_tr_b16 v[90:91], v8 offset:8704\n"
        "ds_read_b64_tr_b16 v[92:93], v9 offset:8192\n"
        "ds_read_b64_tr_b16 v[94:95], v9 offset:8704\n"
        "ds_read_b64_tr_b16 v[96:97], v10 offset:8192\n"
        "ds_read_b64_tr_b16 v[98:99], v10 offset:8704\n"
        "ds_read_b64_tr_b16 v[100:101], v11 offset:8192\n"
        "ds_read_b64_tr_b16 v[102:103], v11 offset:8704\n"
        "ds_read_b64_tr_b16 v[104:105], v8 offset:12288\n"
        "ds_read_b64_tr_b16 v[106:107], v8 offset:12800\n"
        "ds_read_b64_tr_b16 v[108:109], v9 offset:12288\n"
        "ds_read_b64_tr_b16 v[110:111], v9 offset:12800\n"
        "ds_read_b64_tr_b16 v[112:113], v10 offset:12288\n"
        "ds_read_b64_tr_b16 v[114:115], v10 offset:12800\n"
        "ds_read_b64_tr_b16 v[116:117], v11 offset:12288\n"
        "ds_read_b64_tr_b16 v[118:119], v11 offset:12800\n"
        "ds_read_b64_tr_b16 v[120:121], v8 offset:16384\n"
        "ds_read_b64_tr_b16 v[122:123], v8 offset:16896\n"
        "ds_read_b64_tr_b16 v[124:125], v9 offset:16384\n"
        "ds_read_b64_tr_b16 v[126:127], v9 offset:16896\n"
        "ds_read_b64_tr_b16 v[128:129], v10 offset:16384\n"
        "ds_read_b64_tr_b16 v[130:131], v10 offset:16896\n"
        "ds_read_b64_tr_b16 v[132:133], v11 offset:16384\n"
        "ds_read_b64_tr_b16 v[134:135], v11 offset:16896\n"
        "ds_read_b64_tr_b16 v[136:137], v8 offset:20480\n"
        "ds_read_b64_tr_b16 v[138:139], v8 offset:20992\n"
        "ds_read_b64_tr_b16 v[140:141], v9 offset:20480\n"
        "ds_read_b64_tr_b16 v[142:143], v9 offset:20992\n"
        "ds_read_b64_tr_b16 v[144:145], v10 offset:20480\n"
        "ds_read_b64_tr_b16 v[146:147], v10 offset:20992\n"
        "ds_read_b64_tr_b16 v[148:149], v11 offset:20480\n"
        "ds_read_b64_tr_b16 v[150:151], v11 offset:20992\n"
        "ds_read_b64_tr_b16 v[152:153], v8 offset:24576\n"
        "ds_read_b64_tr_b16 v[154:155], v8 offset:25088\n"
        "ds_read_b64_tr_b16 v[156:157], v9 offset:24576\n"
        "ds_read_b64_tr_b16 v[158:159], v9 offset:25088\n"
        "ds_read_b64_tr_b16 v[160:161], v10 offset:24576\n"
        "ds_read_b64_tr_b16 v[162:163], v10 offset:25088\n"
        "ds_read_b64_tr_b16 v[164:165], v11 offset:24576\n"
        "ds_read_b64_tr_b16 v[166:167], v11 offset:25088\n"
        "s_mov_b64 exec, 1\n"
        "ds_add_u32 v4, v5 offset:4\n"
        "s_mov_b64 exec, -1\n"
        "s_lshl2_add_u32 s78, s47, s46\n"
        "s_lshl3_add_u32 s75, s54, s78\n"
        "s_mul_i32 s83, s75, 66560\n"
        "s_lshl_b32 s76, s53, 8\n"
        "s_add_u32 s83, s83, s76\n"
        "s_mul_i32 s76, s50, 0\n"
        "s_sub_u32 s83, s83, s76\n"
        "s_add_u32 s83, s83, 2166784\n"
        "s_lshl2_add_u32 s96, s53, s46\n"
        "s_lshl_b32 s96, s96, 14\n"
        "s_lshl1_add_u32 s76, s54, s47\n"
        "s_lshl_b32 s76, s76, 6\n"
        "s_add_u32 s96, s96, s76\n"
        "v_and_b32 v170, 3, v0\n"
        "v_bfe_u32 v189, v0, 2, 1\n"
        "v_lshrrev_b32 v178, 3, v0\n"
        "v_mul_u32_u24 v179, 8320, v178\n"
        "v_lshl_add_u32 v179, v170, 4, v179\n"
        "v_lshl_add_u32 v171, v189, 7, v179\n"
        "v_lshl_add_u32 v178, v178, 2, v170\n"
        "v_lshl_add_u32 v178, v178, 1, v189\n"
        "v_lshlrev_b32 v178, 4, v178\n"
        "s_and_b32 s76, s46, 1\n"
        "s_lshl_b32 s77, s76, 1\n"
        "v_and_b32 v179, 1, v2\n"
        "v_or_b32 v172, s77, v179\n"
        "v_lshlrev_b32 v172, 5, v172\n"
        "v_xor_b32 v172, v172, v178\n"
        "s_lshl_b32 s77, s78, 10\n"
        "v_add_u32 v172, s77, v172\n"
        "v_add_u32 v176, s81, v3\n"
        "s_lshr_b32 s77, s46, 1\n"
        "s_lshl1_add_u32 s77, s77, s47\n"
        "s_lshl2_add_u32 s77, s77, s76\n"
        "s_lshl_b32 s77, s77, 10\n"
        "v_lshrrev_b32 v178, 1, v2\n"
        "v_lshlrev_b32 v178, 11, v178\n"
        "v_lshl_add_u32 v178, v179, 9, v178\n"
        "v_lshl_add_u32 v178, v1, 4, v178\n"
        "v_add_u32 v173, s77, v178\n"
        "v_add_u32 v177, 45056, v173\n"
        "s_lshl_b32 s76, s46, 5\n"
        "s_lshl_b32 s77, s47, 12\n"
        "v_and_b32 v178, 0xfff, v8\n"
        "v_xor_b32 v178, s76, v178\n"
        "v_add_u32 v174, s77, v178\n"
        "v_lshlrev_b32 v178, 10, v1\n"
        "v_lshl_add_u32 v175, v2, 4, v178\n"
        "v_lshlrev_b32 v188, 3, v1\n"
        "v_and_b32 v178, 0x3c, v0\n"
        "v_lshl_add_u32 v189, v170, 6, v178\n"
        "v_lshlrev_b32 v178, 8, v178\n"
        "v_lshl_add_u32 v175, v170, 4, v178\n"
        "s_and_b32 s33, s42, s36\n"
        "s_cbranch_scc0 .Lcpd_19_%=\n"
        ".Lcp_18_%=:\n"
        "s_and_b64 exec, s[42:43], s[38:39]\n"
        "buffer_load_dwordx2 v[14:15], v255, s[16:19], s32 offen sc1\n"
        "s_waitcnt vmcnt(0)\n"
        "v_cmp_ne_u64 s[42:43], s[62:63], v[14:15]\n"
        "s_mov_b64 exec, -1\n"
        "s_and_b32 s33, s42, s36\n"
        "s_cbranch_scc0 .Lcpd_19_%=\n"
        "s_sleep 1\n"
        "s_add_u32 s61, s61, 16\n"
        "s_cmp_lt_u32 s61, 0x100000\n"
        "s_cbranch_scc1 .Lcp_18_%=\n"
        ".Lcpd_19_%=:\n"
        "s_waitcnt vmcnt(4)\n"
        "buffer_load_dwordx4 v[216:219], v12, s[16:19], s67 offen sc1\n"
        "buffer_load_dwordx4 v[220:223], v12, s[16:19], s67 offen offset:256 sc1\n"
        "s_waitcnt lgkmcnt(0)\n"
        "v_mfma_f32_16x16x32_bf16 v[24:27], v[56:59], v[192:195], 0\n"
        "v_mfma_f32_16x16x32_bf16 v[28:31], v[56:59], v[196:199], 0\n"
        "v_mfma_f32_16x16x32_bf16 v[36:39], v[60:63], v[196:199], 0\n"
        "v_mfma_f32_16x16x32_bf16 v[32:35], v[60:63], v[192:195], 0\n"
        "v_mfma_f32_16x16x32_bf16 v[40:43], v[64:67], v[192:195], 0\n"
        "v_mfma_f32_16x16x32_bf16 v[44:47], v[64:67], v[196:199], 0\n"
        "v_mfma_f32_16x16x32_bf16 v[52:55], v[68:71], v[196:199], 0\n"
        "v_mfma_f32_16x16x32_bf16 v[48:51], v[68:71], v[192:195], 0\n"
        "s_waitcnt vmcnt(4)\n"
        "buffer_load_dwordx4 v[192:195], v12, s[16:19], s68 offen sc1\n"
        "buffer_load_dwordx4 v[196:199], v12, s[16:19], s68 offen offset:256 sc1\n"
        "v_mfma_f32_16x16x32_bf16 v[24:27], v[72:75], v[200:203], v[24:27]\n"
        "ds_read_b64_tr_b16 v[56:57], v8 offset:28672\n"
        "v_mfma_f32_16x16x32_bf16 v[28:31], v[72:75], v[204:207], v[28:31]\n"
        "ds_read_b64_tr_b16 v[58:59], v8 offset:29184\n"
        "v_mfma_f32_16x16x32_bf16 v[36:39], v[76:79], v[204:207], v[36:39]\n"
        "ds_read_b64_tr_b16 v[60:61], v9 offset:28672\n"
        "v_mfma_f32_16x16x32_bf16 v[32:35], v[76:79], v[200:203], v[32:35]\n"
        "ds_read_b64_tr_b16 v[62:63], v9 offset:29184\n"
        "v_mfma_f32_16x16x32_bf16 v[40:43], v[80:83], v[200:203], v[40:43]\n"
        "ds_read_b64_tr_b16 v[64:65], v10 offset:28672\n"
        "v_mfma_f32_16x16x32_bf16 v[44:47], v[80:83], v[204:207], v[44:47]\n"
        "ds_read_b64_tr_b16 v[66:67], v10 offset:29184\n"
        "v_mfma_f32_16x16x32_bf16 v[52:55], v[84:87], v[204:207], v[52:55]\n"
        "ds_read_b64_tr_b16 v[68:69], v11 offset:28672\n"
        "v_mfma_f32_16x16x32_bf16 v[48:51], v[84:87], v[200:203], v[48:51]\n"
        "ds_read_b64_tr_b16 v[70:71], v11 offset:29184\n"
        "s_waitcnt vmcnt(4)\n"
        "buffer_load_dwordx4 v[200:203], v12, s[16:19], s69 offen sc1\n"
        "buffer_load_dwordx4 v[204:207], v12, s[16:19], s69 offen offset:256 sc1\n"
        "s_and_b32 s33, s42, s38\n"
        "s_cbranch_scc0 .Lcpd_21_%=\n"
        ".Lcp_20_%=:\n"
        "s_and_b64 exec, s[42:43], s[38:39]\n"
        "buffer_load_dwordx2 v[14:15], v255, s[16:19], s32 offen sc1\n"
        "s_waitcnt vmcnt(0)\n"
        "v_cmp_ne_u64 s[42:43], s[62:63], v[14:15]\n"
        "s_mov_b64 exec, -1\n"
        "s_and_b32 s33, s42, s38\n"
        "s_cbranch_scc0 .Lcpd_21_%=\n"
        "s_sleep 1\n"
        "s_add_u32 s61, s61, 16\n"
        "s_cmp_lt_u32 s61, 0x100000\n"
        "s_cbranch_scc1 .Lcp_20_%=\n"
        ".Lcpd_21_%=:\n"
        "v_mfma_f32_16x16x32_bf16 v[24:27], v[88:91], v[208:211], v[24:27]\n"
        "ds_read_b64_tr_b16 v[72:73], v8 offset:32768\n"
        "v_mfma_f32_16x16x32_bf16 v[28:31], v[88:91], v[212:215], v[28:31]\n"
        "ds_read_b64_tr_b16 v[74:75], v8 offset:33280\n"
        "v_mfma_f32_16x16x32_bf16 v[36:39], v[92:95], v[212:215], v[36:39]\n"
        "ds_read_b64_tr_b16 v[76:77], v9 offset:32768\n"
        "v_mfma_f32_16x16x32_bf16 v[32:35], v[92:95], v[208:211], v[32:35]\n"
        "ds_read_b64_tr_b16 v[78:79], v9 offset:33280\n"
        "v_mfma_f32_16x16x32_bf16 v[40:43], v[96:99], v[208:211], v[40:43]\n"
        "ds_read_b64_tr_b16 v[80:81], v10 offset:32768\n"
        "v_mfma_f32_16x16x32_bf16 v[44:47], v[96:99], v[212:215], v[44:47]\n"
        "ds_read_b64_tr_b16 v[82:83], v10 offset:33280\n"
        "v_mfma_f32_16x16x32_bf16 v[52:55], v[100:103], v[212:215], v[52:55]\n"
        "ds_read_b64_tr_b16 v[84:85], v11 offset:32768\n"
        "v_mfma_f32_16x16x32_bf16 v[48:51], v[100:103], v[208:211], v[48:51]\n"
        "ds_read_b64_tr_b16 v[86:87], v11 offset:33280\n"
        "s_waitcnt vmcnt(4)\n"
        "buffer_load_dwordx4 v[208:211], v12, s[16:19], s70 offen sc1\n"
        "buffer_load_dwordx4 v[212:215], v12, s[16:19], s70 offen offset:256 sc1\n"
        "v_mfma_f32_16x16x32_bf16 v[24:27], v[104:107], v[216:219], v[24:27]\n"
        "ds_read_b64_tr_b16 v[88:89], v8 offset:36864\n"
        "v_mfma_f32_16x16x32_bf16 v[28:31], v[104:107], v[220:223], v[28:31]\n"
        "ds_read_b64_tr_b16 v[90:91], v8 offset:37376\n"
        "v_mfma_f32_16x16x32_bf16 v[36:39], v[108:111], v[220:223], v[36:39]\n"
        "ds_read_b64_tr_b16 v[92:93], v9 offset:36864\n"
        "v_mfma_f32_16x16x32_bf16 v[32:35], v[108:111], v[216:219], v[32:35]\n"
        "ds_read_b64_tr_b16 v[94:95], v9 offset:37376\n"
        "v_mfma_f32_16x16x32_bf16 v[40:43], v[112:115], v[216:219], v[40:43]\n"
        "ds_read_b64_tr_b16 v[96:97], v10 offset:36864\n"
        "v_mfma_f32_16x16x32_bf16 v[44:47], v[112:115], v[220:223], v[44:47]\n"
        "ds_read_b64_tr_b16 v[98:99], v10 offset:37376\n"
        "v_mfma_f32_16x16x32_bf16 v[52:55], v[116:119], v[220:223], v[52:55]\n"
        "ds_read_b64_tr_b16 v[100:101], v11 offset:36864\n"
        "v_mfma_f32_16x16x32_bf16 v[48:51], v[116:119], v[216:219], v[48:51]\n"
        "ds_read_b64_tr_b16 v[102:103], v11 offset:37376\n"
        "s_waitcnt vmcnt(4)\n"
        "buffer_load_dwordx4 v[216:219], v12, s[16:19], s71 offen sc1\n"
        "buffer_load_dwordx4 v[220:223], v12, s[16:19], s71 offen offset:256 sc1\n"
        "v_mfma_f32_16x16x32_bf16 v[24:27], v[120:123], v[192:195], v[24:27]\n"
        "ds_read_b64_tr_b16 v[104:105], v8 offset:40960\n"
        "v_mfma_f32_16x16x32_bf16 v[28:31], v[120:123], v[196:199], v[28:31]\n"
        "ds_read_b64_tr_b16 v[106:107], v8 offset:41472\n"
        "v_mfma_f32_16x16x32_bf16 v[36:39], v[124:127], v[196:199], v[36:39]\n"
        "ds_read_b64_tr_b16 v[108:109], v9 offset:40960\n"
        "v_mfma_f32_16x16x32_bf16 v[32:35], v[124:127], v[192:195], v[32:35]\n"
        "ds_read_b64_tr_b16 v[110:111], v9 offset:41472\n"
        "v_mfma_f32_16x16x32_bf16 v[40:43], v[128:131], v[192:195], v[40:43]\n"
        "ds_read_b64_tr_b16 v[112:113], v10 offset:40960\n"
        "v_mfma_f32_16x16x32_bf16 v[44:47], v[128:131], v[196:199], v[44:47]\n"
        "ds_read_b64_tr_b16 v[114:115], v10 offset:41472\n"
        "v_mfma_f32_16x16x32_bf16 v[52:55], v[132:135], v[196:199], v[52:55]\n"
        "ds_read_b64_tr_b16 v[116:117], v11 offset:40960\n"
        "v_mfma_f32_16x16x32_bf16 v[48:51], v[132:135], v[192:195], v[48:51]\n"
        "ds_read_b64_tr_b16 v[118:119], v11 offset:41472\n"
        "s_waitcnt vmcnt(4)\n"
        "buffer_load_dwordx4 v[192:195], v12, s[16:19], s72 offen sc1\n"
        "buffer_load_dwordx4 v[196:199], v12, s[16:19], s72 offen offset:256 sc1\n"
        "v_mfma_f32_16x16x32_bf16 v[24:27], v[136:139], v[200:203], v[24:27]\n"
        "v_mfma_f32_16x16x32_bf16 v[28:31], v[136:139], v[204:207], v[28:31]\n"
        "v_mfma_f32_16x16x32_bf16 v[36:39], v[140:143], v[204:207], v[36:39]\n"
        "v_mfma_f32_16x16x32_bf16 v[32:35], v[140:143], v[200:203], v[32:35]\n"
        "v_mfma_f32_16x16x32_bf16 v[40:43], v[144:147], v[200:203], v[40:43]\n"
        "v_mfma_f32_16x16x32_bf16 v[44:47], v[144:147], v[204:207], v[44:47]\n"
        "v_mfma_f32_16x16x32_bf16 v[52:55], v[148:151], v[204:207], v[52:55]\n"
        "v_mfma_f32_16x16x32_bf16 v[48:51], v[148:151], v[200:203], v[48:51]\n"
        "s_waitcnt vmcnt(4)\n"
        "buffer_load_dwordx4 v[200:203], v12, s[16:19], s73 offen sc1\n"
        "buffer_load_dwordx4 v[204:207], v12, s[16:19], s73 offen offset:256 sc1\n"
        "v_mfma_f32_16x16x32_bf16 v[24:27], v[152:155], v[208:211], v[24:27]\n"
        "v_mfma_f32_16x16x32_bf16 v[28:31], v[152:155], v[212:215], v[28:31]\n"
        "v_mfma_f32_16x16x32_bf16 v[36:39], v[156:159], v[212:215], v[36:39]\n"
        "v_mfma_f32_16x16x32_bf16 v[32:35], v[156:159], v[208:211], v[32:35]\n"
        "v_mfma_f32_16x16x32_bf16 v[40:43], v[160:163], v[208:211], v[40:43]\n"
        "v_mfma_f32_16x16x32_bf16 v[44:47], v[160:163], v[212:215], v[44:47]\n"
        "v_mfma_f32_16x16x32_bf16 v[52:55], v[164:167], v[212:215], v[52:55]\n"
        "v_mfma_f32_16x16x32_bf16 v[48:51], v[164:167], v[208:211], v[48:51]\n"
        "s_waitcnt vmcnt(4)\n"
        "buffer_load_dwordx4 v[208:211], v12, s[16:19], s74 offen sc1\n"
        "buffer_load_dwordx4 v[212:215], v12, s[16:19], s74 offen offset:256 sc1\n"
        "s_waitcnt lgkmcnt(0)\n"
        "v_mfma_f32_16x16x32_bf16 v[24:27], v[56:59], v[216:219], v[24:27]\n"
        "v_mfma_f32_16x16x32_bf16 v[28:31], v[56:59], v[220:223], v[28:31]\n"
        "v_mfma_f32_16x16x32_bf16 v[36:39], v[60:63], v[220:223], v[36:39]\n"
        "v_mfma_f32_16x16x32_bf16 v[32:35], v[60:63], v[216:219], v[32:35]\n"
        "v_mfma_f32_16x16x32_bf16 v[40:43], v[64:67], v[216:219], v[40:43]\n"
        "v_mfma_f32_16x16x32_bf16 v[44:47], v[64:67], v[220:223], v[44:47]\n"
        "v_mfma_f32_16x16x32_bf16 v[52:55], v[68:71], v[220:223], v[52:55]\n"
        "v_mfma_f32_16x16x32_bf16 v[48:51], v[68:71], v[216:219], v[48:51]\n"
        "s_waitcnt vmcnt(4)\n"
        "v_mfma_f32_16x16x32_bf16 v[24:27], v[72:75], v[192:195], v[24:27]\n"
        "v_mfma_f32_16x16x32_bf16 v[28:31], v[72:75], v[196:199], v[28:31]\n"
        "v_mfma_f32_16x16x32_bf16 v[36:39], v[76:79], v[196:199], v[36:39]\n"
        "v_mfma_f32_16x16x32_bf16 v[32:35], v[76:79], v[192:195], v[32:35]\n"
        "v_mfma_f32_16x16x32_bf16 v[40:43], v[80:83], v[192:195], v[40:43]\n"
        "v_mfma_f32_16x16x32_bf16 v[44:47], v[80:83], v[196:199], v[44:47]\n"
        "v_mfma_f32_16x16x32_bf16 v[52:55], v[84:87], v[196:199], v[52:55]\n"
        "v_mfma_f32_16x16x32_bf16 v[48:51], v[84:87], v[192:195], v[48:51]\n"
        "s_waitcnt vmcnt(2)\n"
        "v_mfma_f32_16x16x32_bf16 v[24:27], v[88:91], v[200:203], v[24:27]\n"
        "v_mfma_f32_16x16x32_bf16 v[28:31], v[88:91], v[204:207], v[28:31]\n"
        "v_mfma_f32_16x16x32_bf16 v[36:39], v[92:95], v[204:207], v[36:39]\n"
        "v_mfma_f32_16x16x32_bf16 v[32:35], v[92:95], v[200:203], v[32:35]\n"
        "v_mfma_f32_16x16x32_bf16 v[40:43], v[96:99], v[200:203], v[40:43]\n"
        "v_mfma_f32_16x16x32_bf16 v[44:47], v[96:99], v[204:207], v[44:47]\n"
        "v_mfma_f32_16x16x32_bf16 v[52:55], v[100:103], v[204:207], v[52:55]\n"
        "v_mfma_f32_16x16x32_bf16 v[48:51], v[100:103], v[200:203], v[48:51]\n"
        "s_waitcnt vmcnt(0)\n"
        "buffer_load_dwordx4 v[180:183], v171, s[16:19], s83 offen sc1\n"
        "buffer_load_dwordx4 v[184:187], v171, s[16:19], s83 offen offset:64 sc1\n"
        "buffer_load_dwordx2 v[168:169], v188, s[16:19], s49 offen sc1\n"
        "v_mfma_f32_16x16x32_bf16 v[24:27], v[104:107], v[208:211], v[24:27]\n"
        "v_mfma_f32_16x16x32_bf16 v[28:31], v[104:107], v[212:215], v[28:31]\n"
        "v_mfma_f32_16x16x32_bf16 v[36:39], v[108:111], v[212:215], v[36:39]\n"
        "v_mfma_f32_16x16x32_bf16 v[32:35], v[108:111], v[208:211], v[32:35]\n"
        "v_mfma_f32_16x16x32_bf16 v[40:43], v[112:115], v[208:211], v[40:43]\n"
        "v_mfma_f32_16x16x32_bf16 v[44:47], v[112:115], v[212:215], v[44:47]\n"
        "v_mfma_f32_16x16x32_bf16 v[52:55], v[116:119], v[212:215], v[52:55]\n"
        "v_mfma_f32_16x16x32_bf16 v[48:51], v[116:119], v[208:211], v[48:51]\n"
        "s_branch .Lg2e_%=\n"
        ".Lalt_2_%=:\n"
        "s_cmp_eq_u32 s56, 0\n"
        "s_cbranch_scc1 .Lback_3_%=\n"
        "ds_read_b128 v[240:243], v162\n"
        "v_add_u32 v162, 64, v162\n"
        "s_branch .Lfull_4_%=\n"
        ".Lalt_5_%=:\n"
        "s_cmp_eq_u32 s56, 0\n"
        "s_cbranch_scc1 .Lback_6_%=\n"
        "ds_read_b128 v[200:203], v162\n"
        "v_add_u32 v162, 64, v162\n"
        "s_branch .Lfull_7_%=\n"
        ".Lalt_8_%=:\n"
        "s_cmp_eq_u32 s56, 0\n"
        "s_cbranch_scc1 .Lback_9_%=\n"
        "ds_read_b128 v[240:243], v162\n"
        "v_add_u32 v162, 64, v162\n"
        "s_branch .Lfull_10_%=\n"
        ".Lalt_11_%=:\n"
        "s_nop 3\n"
        "s_cmp_eq_u32 s56, 0\n"
        "s_cbranch_scc1 .Lback_12_%=\n"
        "s_branch .Lfull_13_%=\n"
        ".Lgate_%=:\n"
        "s_cmp_eq_u32 s56, 0\n"
        "s_cbranch_scc1 .Lepid_%=\n"
        "v_permlane16_swap_b32 v120, v124\n"
        "v_permlane16_swap_b32 v128, v132\n"
        "s_nop 1\n"
        "v_permlane32_swap_b32 v120, v128\n"
        "v_lshlrev_b32 v13, 3, v0\n"
        "v_mov_b32 v121, s62\n"
        "s_lshl_b32 s75, s48, 9\n"
        "s_add_u32 s75, s75, 12288\n"
        "v_cvt_pk_bf16_f32 v120, v120, v120\n"
        "v_lshlrev_b32 v120, 16, v120\n"
        "v_mul_f32 v120, 0xbfb8aa3b, v120\n"
        "v_exp_f32 v120, v120\n"
        "s_nop 0\n"
        "v_add_f32 v120, 1.0, v120\n"
        "v_rcp_f32 v120, v120\n"
        "s_nop 0\n"
        "v_cvt_pk_bf16_f32 v120, v120, v120\n"
        "v_lshlrev_b32 v120, 16, v120\n"
        "buffer_store_dwordx2 v[120:121], v13, s[16:19], s75 offen sc1\n"
        "s_branch .Lepid_%=\n"
        ".Lssm_%=:\n"
        "s_waitcnt lgkmcnt(0)\n"
        "s_barrier\n"
        "s_setprio 0\n"
        "s_add_u32 m0, s60, 139264\n"
        "s_nop 0\n"
        "buffer_load_dwordx4 v126, s[8:11], s13 offen offset:2048 lds\n"
        "ds_read_b128 v[192:195], v140 offset:0\n"
        "ds_read_b128 v[196:199], v140 offset:8704\n"
        "ds_read_b128 v[200:203], v140 offset:64\n"
        "ds_read_b128 v[204:207], v140 offset:8768\n"
        "s_add_u32 m0, s60, 145408\n"
        "s_nop 0\n"
        "buffer_load_dwordx4 v124, s[8:11], s14 offen offset:0 lds\n"
        "ds_read_b128 v[208:211], v140 offset:128\n"
        "ds_read_b128 v[212:215], v140 offset:8832\n"
        "ds_read_b128 v[216:219], v140 offset:192\n"
        "ds_read_b128 v[220:223], v140 offset:8896\n"
        "buffer_load_dwordx4 v125, s[8:11], s14 offen offset:1024 lds\n"
        "ds_read_b128 v[224:227], v140 offset:256\n"
        "ds_read_b128 v[228:231], v140 offset:8960\n"
        "ds_read_b128 v[232:235], v140 offset:320\n"
        "buffer_load_dwordx4 v126, s[8:11], s14 offen offset:2048 lds\n"
        "ds_read_b128 v[236:239], v140 offset:9024\n"
        "ds_read_b128 v[240:243], v140 offset:384\n"
        "ds_read_b128 v[244:247], v140 offset:9088\n"
        "ds_read_b128 v[248:251], v140 offset:448\n"
        "s_add_u32 m0, s59, 40960\n"
        "s_add_u32 s58, s58, 128\n"
        "s_and_b32 s58, s58, 1023\n"
        "s_add_u32 s12, s56, s58\n"
        "buffer_load_dwordx4 v120, s[4:7], s12 offen offset:0 lds\n"
        "s_mul_i32 s13, s58, 1376\n"
        "ds_read_b128 v[252:255], v140 offset:9152\n"
        "ds_read_b128 v[56:59], v141 offset:64\n"
        "ds_read_b128 v[60:63], v141 offset:128\n"
        "ds_read_b128 v[64:67], v141 offset:192\n"
        "buffer_load_dwordx4 v121, s[4:7], s12 offen offset:1024 lds\n"
        "s_add_u32 s13, s13, s57\n"
        "ds_read_b128 v[68:71], v141 offset:256\n"
        "ds_read_b128 v[72:75], v141 offset:320\n"
        "ds_read_b128 v[76:79], v141 offset:384\n"
        "buffer_load_dwordx4 v122, s[4:7], s12 offen offset:2048 lds\n"
        "s_add_u32 s14, s13, 88064\n"
        "ds_read_b128 v[80:83], v141 offset:448\n"
        "ds_read_b128 v[84:87], v141 offset:512\n"
        "v_mov_b32 v4, 0\n"
        "v_mov_b32 v5, 1\n"
        "buffer_load_dwordx4 v123, s[4:7], s12 offen offset:3072 lds\n"
        "v_mov_b32 v6, s62\n"
        "v_mov_b32 v7, s63\n"
        "s_mov_b32 s61, 0\n"
        "s_waitcnt lgkmcnt(0)\n"
        "s_barrier\n"
        "s_setprio 2\n"
        "v_mfma_f32_16x16x32_bf16 v[112:115], v[192:195], v[56:59], v[112:115]\n"
        "v_mfma_f32_16x16x32_bf16 v[116:119], v[196:199], v[56:59], 0\n"
        "s_waitcnt vmcnt(14)\n"
        "s_mov_b64 exec, 1\n"
        "buffer_store_dwordx2 v[6:7], off, s[16:19], s48 sc1\n"
        "s_mov_b64 exec, -1\n"
        "s_add_u32 m0, s60, 57344\n"
        "s_nop 0\n"
        "buffer_load_dwordx4 v124, s[8:11], s13 offen offset:0 lds\n"
        "v_mfma_f32_16x16x32_bf16 v[112:115], v[200:203], v[60:63], v[112:115]\n"
        "v_mfma_f32_16x16x32_bf16 v[116:119], v[204:207], v[60:63], v[116:119]\n"
        "v_mfma_f32_16x16x32_bf16 v[112:115], v[208:211], v[64:67], v[112:115]\n"
        "buffer_load_dwordx4 v125, s[8:11], s13 offen offset:1024 lds\n"
        "v_mfma_f32_16x16x32_bf16 v[116:119], v[212:215], v[64:67], v[116:119]\n"
        "v_mfma_f32_16x16x32_bf16 v[112:115], v[216:219], v[68:71], v[112:115]\n"
        "buffer_load_dwordx4 v126, s[8:11], s13 offen offset:2048 lds\n"
        "v_mfma_f32_16x16x32_bf16 v[116:119], v[220:223], v[68:71], v[116:119]\n"
        "v_mfma_f32_16x16x32_bf16 v[112:115], v[224:227], v[72:75], v[112:115]\n"
        "s_add_u32 m0, s60, 63488\n"
        "s_nop 0\n"
        "buffer_load_dwordx4 v124, s[8:11], s14 offen offset:0 lds\n"
        "v_mfma_f32_16x16x32_bf16 v[116:119], v[228:231], v[72:75], v[116:119]\n"
        "v_mfma_f32_16x16x32_bf16 v[112:115], v[232:235], v[76:79], v[112:115]\n"
        "v_mfma_f32_16x16x32_bf16 v[116:119], v[236:239], v[76:79], v[116:119]\n"
        "buffer_load_dwordx4 v125, s[8:11], s14 offen offset:1024 lds\n"
        "v_mfma_f32_16x16x32_bf16 v[112:115], v[240:243], v[80:83], v[112:115]\n"
        "v_mfma_f32_16x16x32_bf16 v[116:119], v[244:247], v[80:83], v[116:119]\n"
        "buffer_load_dwordx4 v126, s[8:11], s14 offen offset:2048 lds\n"
        "v_mfma_f32_16x16x32_bf16 v[112:115], v[248:251], v[84:87], v[112:115]\n"
        "v_mfma_f32_16x16x32_bf16 v[116:119], v[252:255], v[84:87], v[116:119]\n"
        "s_nop 6\n"
        "v_cvt_pk_bf16_f32 v52, v112, v113\n"
        "v_cvt_pk_bf16_f32 v53, v114, v115\n"
        "v_cvt_pk_bf16_f32 v54, v116, v117\n"
        "v_cvt_pk_bf16_f32 v55, v118, v119\n"
        "s_waitcnt vmcnt(7)\n"
        "s_setprio 0\n"
        "ds_write_b16 v136, v52\n"
        "ds_write_b16_d16_hi v136, v52 offset:4\n"
        "ds_write_b16 v137, v53\n"
        "ds_write_b16_d16_hi v137, v53 offset:4\n"
        "ds_write_b16 v138, v54 offset:2048\n"
        "ds_write_b16_d16_hi v138, v54 offset:2176\n"
        "ds_write_b16 v138, v55 offset:2304\n"
        "ds_write_b16_d16_hi v138, v55 offset:2432\n"
        "s_setprio 0\n"
        "s_barrier\n"
        "s_add_u32 m0, s59, 81920\n"
        "s_add_u32 s58, s58, 128\n"
        "s_and_b32 s58, s58, 1023\n"
        "s_add_u32 s12, s56, s58\n"
        "buffer_load_dwordx4 v120, s[4:7], s12 offen offset:0 lds\n"
        "s_mul_i32 s13, s58, 1376\n"
        "s_lshl_b32 s75, s46, 10\n"
        "v_add_u32 v145, s75, v3\n"
        "v_lshrrev_b32 v26, 1, v145\n"
        "v_sub_u32 v26, 2047, v26\n"
        "v_cvt_f32_i32 v156, v26\n"
        "buffer_load_dwordx4 v121, s[4:7], s12 offen offset:1024 lds\n"
        "s_add_u32 s13, s13, s57\n"
        "v_lshlrev_b32 v26, 2, v26\n"
        "v_add_u32 v26, 35968, v26\n"
        "v_mov_b32 v27, -32128\n"
        "v_mov_b32 v158, 36480\n"
        "v_add_f32 v157, -1.0, v156\n"
        "buffer_load_dwordx4 v122, s[4:7], s12 offen offset:2048 lds\n"
        "s_add_u32 s14, s13, 88064\n"
        "v_cvt_pk_bf16_f32 v159, v156, v157\n"
        "v_add_f32 v156, -2.0, v156\n"
        "v_mad_i32_i16 v157, v159, 4, v27\n"
        "v_med3_i32 v127, v26, v157, v158\n"
        "v_add_u32 v26, -4, v26\n"
        "buffer_load_dwordx4 v123, s[4:7], s12 offen offset:3072 lds\n"
        "v_mad_i32_i16 v157, v159, 4, v27 op_sel:[1,0,0,0]\n"
        "v_med3_i32 v128, v26, v157, v158\n"
        "v_add_u32 v26, -4, v26\n"
        "v_add_f32 v157, -1.0, v156\n"
        "v_cvt_pk_bf16_f32 v159, v156, v157\n"
        "s_add_u32 m0, s60, 98304\n"
        "s_nop 0\n"
        "buffer_load_dwordx4 v124, s[8:11], s13 offen offset:0 lds\n"
        "v_add_f32 v156, -2.0, v156\n"
        "v_mad_i32_i16 v157, v159, 4, v27\n"
        "v_med3_i32 v129, v26, v157, v158\n"
        "v_add_u32 v26, -4, v26\n"
        "v_mad_i32_i16 v157, v159, 4, v27 op_sel:[1,0,0,0]\n"
        "buffer_load_dwordx4 v125, s[8:11], s13 offen offset:1024 lds\n"
        "v_med3_i32 v130, v26, v157, v158\n"
        "v_add_u32 v26, -4, v26\n"
        "v_add_f32 v157, -1.0, v156\n"
        "v_cvt_pk_bf16_f32 v159, v156, v157\n"
        "v_add_f32 v156, -2.0, v156\n"
        "buffer_load_dwordx4 v126, s[8:11], s13 offen offset:2048 lds\n"
        "v_mad_i32_i16 v157, v159, 4, v27\n"
        "v_med3_i32 v131, v26, v157, v158\n"
        "v_add_u32 v26, -4, v26\n"
        "v_mad_i32_i16 v157, v159, 4, v27 op_sel:[1,0,0,0]\n"
        "v_med3_i32 v132, v26, v157, v158\n"
        "s_add_u32 m0, s60, 104448\n"
        "s_nop 0\n"
        "buffer_load_dwordx4 v124, s[8:11], s14 offen offset:0 lds\n"
        "v_add_u32 v26, -4, v26\n"
        "v_add_f32 v157, -1.0, v156\n"
        "v_cvt_pk_bf16_f32 v159, v156, v157\n"
        "v_add_f32 v156, -2.0, v156\n"
        "v_mad_i32_i16 v157, v159, 4, v27\n"
        "buffer_load_dwordx4 v125, s[8:11], s14 offen offset:1024 lds\n"
        "v_med3_i32 v133, v26, v157, v158\n"
        "v_add_u32 v26, -4, v26\n"
        "v_mad_i32_i16 v157, v159, 4, v27 op_sel:[1,0,0,0]\n"
        "v_med3_i32 v134, v26, v157, v158\n"
        "v_add_u32 v26, -4, v26\n"
        "buffer_load_dwordx4 v126, s[8:11], s14 offen offset:2048 lds\n"
        "v_add_f32 v157, -1.0, v156\n"
        "v_cvt_pk_bf16_f32 v159, v156, v157\n"
        "v_mad_i32_i16 v157, v159, 4, v27\n"
        "v_med3_i32 v135, v26, v157, v158\n"
        "s_waitcnt lgkmcnt(0)\n"
        "s_waitcnt vmcnt(10)\n"
        "s_barrier\n"
        "s_add_u32 m0, s59, 122880\n"
        "s_add_u32 s58, s58, 128\n"
        "s_and_b32 s58, s58, 1023\n"
        "s_add_u32 s12, s56, s58\n"
        "buffer_load_dwordx4 v120, s[4:7], s12 offen offset:0 lds\n"
        "s_mul_i32 s13, s58, 1376\n"
        "s_and_b32 s75, s50, 1\n"
        "v_and_b32 v26, 1, v2\n"
        "s_mul_i32 s97, s75, 22\n"
        "s_lshl_b32 s77, s83, 1\n"
        "v_or_b32 v26, s77, v26\n"
        "s_lshr_b32 s75, s50, 1\n"
        "buffer_load_dwordx4 v121, s[4:7], s12 offen offset:1024 lds\n"
        "s_add_u32 s13, s13, s57\n"
        "v_lshlrev_b32 v26, 5, v26\n"
        "s_mul_i32 s75, s75, 7\n"
        "v_xor_b32 v26, v26, v3\n"
        "s_add_u32 s97, s97, s75\n"
        "v_and_b32 v26, 0x7f, v26\n"
        "s_mul_i32 s75, s54, 0\n"
        "buffer_load_dwordx4 v122, s[4:7], s12 offen offset:2048 lds\n"
        "s_add_u32 s14, s13, 88064\n"
        "v_lshrrev_b32 v27, 3, v0\n"
        "s_lshr_b32 s75, s75, 1\n"
        "v_lshl_add_u32 v22, v27, 10, v26\n"
        "s_add_u32 s97, s97, s75\n"
        "s_lshl2_add_u32 s80, s97, s46\n"
        "s_lshl_b32 s80, s80, 6\n"
        "buffer_load_dwordx4 v123, s[4:7], s12 offen offset:3072 lds\n"
        "s_add_u32 s80, s80, s54\n"
        "s_lshl_b32 s80, s80, 7\n"
        "s_lshl_b32 s55, s46, 10\n"
        "s_and_b32 s75, s3, 7\n"
        "s_lshl2_add_u32 s53, s75, s50\n"
        "v_lshl_add_u32 v26, v2, 3, v24\n"
        "s_add_u32 m0, s60, 139264\n"
        "s_nop 0\n"
        "buffer_load_dwordx4 v124, s[8:11], s13 offen offset:0 lds\n"
        "v_lshlrev_b32 v26, 7, v26\n"
        "v_lshl_add_u32 v26, v25, 3, v26\n"
        "v_bfe_u32 v27, v0, 3, 2\n"
        "v_lshl_add_u32 v26, v27, 5, v26\n"
        "s_mul_i32 s75, s82, 45056\n"
        "s_add_u32 s75, s75, 45056\n"
        "buffer_load_dwordx4 v125, s[8:11], s13 offen offset:1024 lds\n"
        "v_add_u32 v8, s75, v26\n"
        "v_xor_b32 v9, 32, v8\n"
        "v_xor_b32 v10, 64, v8\n"
        "v_xor_b32 v11, 0x60, v8\n"
        "v_lshlrev_b32 v26, 9, v2\n"
        "v_lshl_add_u32 v12, v1, 4, v26\n"
        "buffer_load_dwordx4 v126, s[8:11], s13 offen offset:2048 lds\n"
        "s_mul_i32 s75, s82, 11\n"
        "s_add_u32 s75, s75, s97\n"
        "s_lshl_b32 s32, s53, 12\n"
        "s_add_u32 s32, s32, 12128256\n"
        "v_lshlrev_b32 v255, 7, v0\n"
        "v_bfe_u32 v26, v0, 1, 4\n"
        "s_add_u32 m0, s60, 145408\n"
        "s_nop 0\n"
        "buffer_load_dwordx4 v124, s[8:11], s14 offen offset:0 lds\n"
        "v_add_u32 v27, 1, v26\n"
        "v_bfm_b32 v27, v27, 0\n"
        "v_and_b32 v27, 0xc888, v27\n"
        "v_bcnt_u32_b32 v13, v27, s75\n"
        "v_lshrrev_b32 v27, v26, v27\n"
        "v_mul_u32_u24 v26, 3, v26\n"
        "buffer_load_dwordx4 v125, s[8:11], s14 offen offset:1024 lds\n"
        "v_sub_u32 v26, v26, v13\n"
        "v_add_u32 v26, 88, v26\n"
        "v_subrev_u32 v13, 43, v26\n"
        "v_min_u32 v26, v26, v13\n"
        "v_subrev_u32 v13, 43, v26\n"
        "v_min_u32 v26, v26, v13\n"
        "buffer_load_dwordx4 v126, s[8:11], s14 offen offset:2048 lds\n"
        "v_and_b32 v13, 32, v0\n"
        "v_add_u32 v26, v26, v27\n"
        "v_lshl_add_u32 v26, v13, 1, v26\n"
        "v_cmp_ge_u32 s[34:35], 4, v26\n"
        "v_cmp_ge_u32 s[36:37], 7, v26\n"
        "v_cmp_ge_u32 s[38:39], 12, v26\n"
        "s_waitcnt vmcnt(10)\n"
        "s_barrier\n"
        "s_add_u32 m0, s59, 40960\n"
        "s_add_u32 s58, s58, 128\n"
        "s_and_b32 s58, s58, 1023\n"
        "s_add_u32 s12, s56, s58\n"
        "buffer_load_dwordx4 v120, s[4:7], s12 offen offset:0 lds\n"
        "s_mul_i32 s13, s58, 1376\n"
        "s_lshl_b32 s75, s75, 11\n"
        "s_lshl1_add_u32 s76, s53, s83\n"
        "s_mul_i32 s76, s76, 89088\n"
        "s_add_u32 s76, s76, 6426624\n"
        "s_sub_u32 s77, s75, 88064\n"
        "s_min_u32 s75, s75, s77\n"
        "s_add_u32 s64, s75, s76\n"
        "s_add_u32 s75, s75, 2048\n"
        "buffer_load_dwordx4 v121, s[4:7], s12 offen offset:1024 lds\n"
        "s_add_u32 s13, s13, s57\n"
        "s_sub_u32 s77, s75, 88064\n"
        "s_min_u32 s75, s75, s77\n"
        "s_add_u32 s65, s75, s76\n"
        "s_add_u32 s75, s75, 2048\n"
        "s_sub_u32 s77, s75, 88064\n"
        "s_min_u32 s75, s75, s77\n"
        "s_add_u32 s66, s75, s76\n"
        "buffer_load_dwordx4 v122, s[4:7], s12 offen offset:2048 lds\n"
        "s_add_u32 s14, s13, 88064\n"
        "s_add_u32 s75, s75, 2048\n"
        "s_sub_u32 s77, s75, 88064\n"
        "s_min_u32 s75, s75, s77\n"
        "s_add_u32 s67, s75, s76\n"
        "s_add_u32 s75, s75, 2048\n"
        "s_sub_u32 s77, s75, 88064\n"
        "s_min_u32 s75, s75, s77\n"
        "buffer_load_dwordx4 v123, s[4:7], s12 offen offset:3072 lds\n"
        "s_add_u32 s68, s75, s76\n"
        "s_add_u32 s75, s75, 2048\n"
        "s_sub_u32 s77, s75, 88064\n"
        "s_min_u32 s75, s75, s77\n"
        "s_add_u32 s69, s75, s76\n"
        "s_add_u32 s75, s75, 2048\n"
        "s_sub_u32 s77, s75, 88064\n"
        "s_min_u32 s75, s75, s77\n"
        "s_add_u32 m0, s60, 57344\n"
        "s_nop 0\n"
        "buffer_load_dwordx4 v124, s[8:11], s13 offen offset:0 lds\n"
        "s_add_u32 s70, s75, s76\n"
        "s_add_u32 s75, s75, 2048\n"
        "s_sub_u32 s77, s75, 88064\n"
        "s_min_u32 s75, s75, s77\n"
        "s_add_u32 s71, s75, s76\n"
        "s_add_u32 s75, s75, 2048\n"
        "s_sub_u32 s77, s75, 88064\n"
        "buffer_load_dwordx4 v125, s[8:11], s13 offen offset:1024 lds\n"
        "s_min_u32 s75, s75, s77\n"
        "s_add_u32 s72, s75, s76\n"
        "s_add_u32 s75, s75, 2048\n"
        "s_sub_u32 s77, s75, 88064\n"
        "s_min_u32 s75, s75, s77\n"
        "s_add_u32 s73, s75, s76\n"
        "s_add_u32 s75, s75, 2048\n"
        "buffer_load_dwordx4 v126, s[8:11], s13 offen offset:2048 lds\n"
        "s_sub_u32 s77, s75, 88064\n"
        "s_min_u32 s75, s75, s77\n"
        "s_add_u32 s74, s75, s76\n"
        "s_cmp_ge_u32 s46, 2\n"
        "s_cselect_b32 s74, 2130706432, s74\n"
        "s_lshl2_add_u32 s49, s53, s46\n"
        "s_lshl_b32 s49, s49, 7\n"
        "s_add_u32 s49, s49, 12288\n"
        "s_add_u32 m0, s60, 63488\n"
        "s_nop 0\n"
        "buffer_load_dwordx4 v124, s[8:11], s14 offen offset:0 lds\n"
        "ds_read_b32 v200, v127\n"
        "ds_read_b32 v201, v128\n"
        "ds_read_b32 v202, v129\n"
        "ds_read_b32 v203, v130\n"
        "ds_read_b32 v204, v131\n"
        "ds_read_b32 v205, v132\n"
        "ds_read_b32 v206, v133\n"
        "buffer_load_dwordx4 v125, s[8:11], s14 offen offset:1024 lds\n"
        "ds_read_b32 v207, v134\n"
        "ds_read_b32 v208, v135\n"
        "s_lshr_b32 s75, s3, 3\n"
        "s_lshl_b32 s75, s75, 8\n"
        "s_add_u32 s48, s75, 4096\n"
        "s_cmp_eq_u32 s3, 0\n"
        "s_cselect_b32 s75, 1024, 0\n"
        "buffer_load_dwordx4 v126, s[8:11], s14 offen offset:2048 lds\n"
        "s_lshl_b32 s76, s46, 1\n"
        "v_and_b32 v157, 1, v2\n"
        "v_add_u32 v26, s76, v157\n"
        "v_min_u32 v27, 1, v2\n"
        "v_mul_u32_u24 v26, v26, v27\n"
        "v_mul_u32_u24 v26, s75, v26\n"
        "v_lshl_add_u32 v149, v1, 4, v26\n"
        "s_waitcnt vmcnt(10)\n"
        "s_barrier\n"
        "buffer_load_dwordx4 v[14:17], v149, s[16:19], s48 offen sc1\n"
        "s_add_u32 m0, s59, 81920\n"
        "s_add_u32 s58, s58, 128\n"
        "s_and_b32 s58, s58, 1023\n"
        "s_add_u32 s12, s56, s58\n"
        "buffer_load_dwordx4 v120, s[4:7], s12 offen offset:0 lds\n"
        "s_mul_i32 s13, s58, 1376\n"
        "v_lshrrev_b32 v26, 3, v0\n"
        "v_and_b32 v27, 7, v0\n"
        "v_and_b32 v156, 3, v26\n"
        "v_mul_u32_u24 v156, 4160, v156\n"
        "v_lshl_add_u32 v146, v27, 2, v156\n"
        "s_mul_i32 s75, s82, 8320\n"
        "buffer_load_dwordx4 v121, s[4:7], s12 offen offset:1024 lds\n"
        "s_add_u32 s13, s13, s57\n"
        "s_lshl_b32 s76, s83, 8\n"
        "s_add_u32 s75, s75, s76\n"
        "s_add_u32 s75, s75, 64\n"
        "v_sub_u32 v156, 15, v1\n"
        "v_and_b32 v158, 1, v156\n"
        "buffer_load_dwordx4 v122, s[4:7], s12 offen offset:2048 lds\n"
        "s_add_u32 s14, s13, 88064\n"
        "v_mul_u32_u24 v158, 4158, v158\n"
        "v_lshl_add_u32 v158, v156, 1, v158\n"
        "v_lshl_add_u32 v158, v2, 4, v158\n"
        "v_add_u32 v248, s75, v158\n"
        "v_add_u32 v249, 1024, v248\n"
        "v_add_u32 v250, 2048, v248\n"
        "buffer_load_dwordx4 v123, s[4:7], s12 offen offset:3072 lds\n"
        "v_add_u32 v251, 3072, v248\n"
        "s_mul_i32 s52, s82, 4576\n"
        "s_add_u32 s52, s52, 16704\n"
        "s_add_u32 s75, s52, s76\n"
        "v_lshlrev_b32 v156, 4, v2\n"
        "s_add_u32 m0, s60, 98304\n"
        "s_nop 0\n"
        "buffer_load_dwordx4 v124, s[8:11], s13 offen offset:0 lds\n"
        "v_lshl_add_u32 v158, v1, 5, v156\n"
        "v_add_u32 v18, s75, v158\n"
        "v_lshl_add_u32 v158, v24, 2, v2\n"
        "v_lshl_add_u32 v158, v158, 2, v25\n"
        "v_lshlrev_b32 v19, 4, v158\n"
        "buffer_load_dwordx4 v125, s[8:11], s13 offen offset:1024 lds\n"
        "s_lshl_b32 s75, s46, 12\n"
        "v_add_u32 v20, s75, v3\n"
        "v_xor_b32 v21, 0x1000, v20\n"
        "v_lshrrev_b32 v156, 2, v0\n"
        "v_lshlrev_b32 v156, 7, v156\n"
        "v_lshl_add_u32 v150, v25, 4, v156\n"
        "buffer_load_dwordx4 v126, s[8:11], s13 offen offset:2048 lds\n"
        "s_mov_b32 s51, 2130706432\n"
        "s_lshl1_add_u32 s75, s3, s82\n"
        "s_mul_i32 s81, s75, 8320\n"
        "s_add_u32 s81, s81, 2166784\n"
        "s_mul_i32 s28, s3, 8320\n"
        "s_add_u32 m0, s60, 104448\n"
        "s_nop 0\n"
        "buffer_load_dwordx4 v124, s[8:11], s14 offen offset:0 lds\n"
        "s_lshl_b32 s75, s83, 12\n"
        "s_add_u32 s28, s28, s75\n"
        "s_lshl_b32 s75, s82, 6\n"
        "s_add_u32 s28, s28, s75\n"
        "s_add_u32 s28, s28, 36864\n"
        "s_add_u32 s29, s28, 1024\n"
        "buffer_load_dwordx4 v125, s[8:11], s14 offen offset:1024 lds\n"
        "s_lshl_b32 s75, s83, 11\n"
        "s_add_u32 s30, s52, s75\n"
        "v_mov_b32 v233, s62\n"
        "v_mov_b32 v235, s62\n"
        "v_mov_b32 v237, s62\n"
        "buffer_load_dwordx4 v126, s[8:11], s14 offen offset:2048 lds\n"
        "v_mov_b32 v239, s62\n"
        "v_mov_b32 v241, s62\n"
        "v_mov_b32 v243, s62\n"
        "v_mov_b32 v245, s62\n"
        "v_mov_b32 v247, s62\n"
        "s_waitcnt vmcnt(11)\n"
        "s_barrier\n"
        "s_add_u32 m0, s59, 122880\n"
        "s_add_u32 s58, s58, 128\n"
        "s_and_b32 s58, s58, 1023\n"
        "s_add_u32 s12, s56, s58\n"
        "buffer_load_dwordx4 v120, s[4:7], s12 offen offset:0 lds\n"
        "s_mul_i32 s13, s58, 1376\n"
        "s_waitcnt lgkmcnt(0)\n"
        "s_mov_b32 s76, 0x05040100\n"
        "buffer_load_dwordx4 v121, s[4:7], s12 offen offset:1024 lds\n"
        "s_add_u32 s13, s13, s57\n"
        "s_mov_b32 s77, 0x07060302\n"
        "v_perm_b32 v168, v201, v200, s76\n"
        "buffer_load_dwordx4 v122, s[4:7], s12 offen offset:2048 lds\n"
        "s_add_u32 s14, s13, 88064\n"
        "v_perm_b32 v169, v203, v202, s76\n"
        "v_perm_b32 v170, v205, v204, s76\n"
        "buffer_load_dwordx4 v123, s[4:7], s12 offen offset:3072 lds\n"
        "v_perm_b32 v171, v207, v206, s76\n"
        "v_perm_b32 v172, v202, v201, s76\n"
        "s_add_u32 m0, s60, 139264\n"
        "s_nop 0\n"
        "buffer_load_dwordx4 v124, s[8:11], s13 offen offset:0 lds\n"
        "v_perm_b32 v173, v204, v203, s76\n"
        "v_perm_b32 v174, v206, v205, s76\n"
        "buffer_load_dwordx4 v125, s[8:11], s13 offen offset:1024 lds\n"
        "v_perm_b32 v175, v208, v207, s76\n"
        "v_perm_b32 v176, v201, v200, s77\n"
        "buffer_load_dwordx4 v126, s[8:11], s13 offen offset:2048 lds\n"
        "v_perm_b32 v177, v203, v202, s77\n"
        "v_perm_b32 v178, v205, v204, s77\n"
        "s_add_u32 m0, s60, 145408\n"
        "s_nop 0\n"
        "buffer_load_dwordx4 v124, s[8:11], s14 offen offset:0 lds\n"
        "v_perm_b32 v179, v207, v206, s77\n"
        "v_perm_b32 v180, v202, v201, s77\n"
        "buffer_load_dwordx4 v125, s[8:11], s14 offen offset:1024 lds\n"
        "v_perm_b32 v181, v204, v203, s77\n"
        "v_perm_b32 v182, v206, v205, s77\n"
        "buffer_load_dwordx4 v126, s[8:11], s14 offen offset:2048 lds\n"
        "v_perm_b32 v183, v208, v207, s77\n"
        "s_waitcnt vmcnt(20)\n"
        ".Lpl_22_%=:\n"
        "v_cmp_ne_u64 vcc, s[62:63], v[14:15]\n"
        "v_cmp_ne_u64 s[76:77], s[62:63], v[16:17]\n"
        "s_or_b64 vcc, vcc, s[76:77]\n"
        "s_cbranch_vccz .Lpld_23_%=\n"
        "s_sleep 1\n"
        "buffer_load_dwordx4 v[14:17], v149, s[16:19], s48 offen sc1\n"
        "s_add_u32 s61, s61, 16\n"
        "s_cmp_lt_u32 s61, 0x100000\n"
        "s_waitcnt vmcnt(0)\n"
        "s_cbranch_scc1 .Lpl_22_%=\n"
        ".Lpld_23_%=:\n"
        "s_mov_b32 m0, s52\n"
        "s_mov_b64 exec, 0x3fffffff\n"
        "buffer_load_dwordx4 v150, s[16:19], s51 offen lds\n"
        "s_add_u32 m0, s30, 480\n"
        "s_mov_b64 exec, -1\n"
        "buffer_load_dwordx4 v150, s[16:19], s28 offen sc1 lds\n"
        "buffer_load_dwordx4 v150, s[16:19], s29 offen offset:1024 sc1 lds\n"
        "s_waitcnt vmcnt(13)\n"
        "s_barrier\n"
        "s_add_u32 m0, s55, 45056\n"
        "s_nop 0\n"
        "buffer_load_dwordx4 v22, s[20:23], s80 offen lds\n"
        "s_add_u32 m0, s55, 49152\n"
        "s_add_u32 s80, s80, 32768\n"
        "s_sub_u32 s15, s80, 1409024\n"
        "s_min_u32 s80, s80, s15\n"
        "buffer_load_dwordx4 v22, s[20:23], s80 offen lds\n"
        "s_add_u32 m0, s55, 53248\n"
        "s_add_u32 s80, s80, 32768\n"
        "s_sub_u32 s15, s80, 1409024\n"
        "s_min_u32 s80, s80, s15\n"
        "buffer_load_dwordx4 v22, s[20:23], s80 offen lds\n"
        "s_add_u32 m0, s55, 57344\n"
        "s_add_u32 s80, s80, 32768\n"
        "s_sub_u32 s15, s80, 1409024\n"
        "s_min_u32 s80, s80, s15\n"
        "buffer_load_dwordx4 v22, s[20:23], s80 offen lds\n"
        "s_add_u32 m0, s55, 61440\n"
        "s_add_u32 s80, s80, 32768\n"
        "s_sub_u32 s15, s80, 1409024\n"
        "s_min_u32 s80, s80, s15\n"
        "buffer_load_dwordx4 v22, s[20:23], s80 offen lds\n"
        "s_add_u32 m0, s55, 65536\n"
        "s_add_u32 s80, s80, 32768\n"
        "s_sub_u32 s15, s80, 1409024\n"
        "s_min_u32 s80, s80, s15\n"
        "buffer_load_dwordx4 v22, s[20:23], s80 offen lds\n"
        "s_add_u32 m0, s55, 69632\n"
        "s_add_u32 s80, s80, 32768\n"
        "s_sub_u32 s15, s80, 1409024\n"
        "s_min_u32 s80, s80, s15\n"
        "buffer_load_dwordx4 v22, s[20:23], s80 offen lds\n"
        "s_add_u32 m0, s55, 73728\n"
        "s_add_u32 s80, s80, 720896\n"
        "s_sub_u32 s15, s80, 1409024\n"
        "s_min_u32 s80, s80, s15\n"
        "buffer_load_dwordx4 v22, s[20:23], s80 offen lds\n"
        "s_add_u32 m0, s55, 77824\n"
        "s_add_u32 s80, s80, 32768\n"
        "s_sub_u32 s15, s80, 1409024\n"
        "s_min_u32 s80, s80, s15\n"
        "buffer_load_dwordx4 v22, s[20:23], s80 offen lds\n"
        "s_waitcnt vmcnt(12)\n"
        "s_barrier\n"
        "s_add_u32 m0, s55, 90112\n"
        "s_add_u32 s80, s80, 819200\n"
        "s_sub_u32 s15, s80, 1409024\n"
        "s_min_u32 s80, s80, s15\n"
        "buffer_load_dwordx4 v22, s[20:23], s80 offen lds\n"
        "s_add_u32 m0, s55, 94208\n"
        "s_add_u32 s80, s80, 32768\n"
        "s_sub_u32 s15, s80, 1409024\n"
        "s_min_u32 s80, s80, s15\n"
        "buffer_load_dwordx4 v22, s[20:23], s80 offen lds\n"
        "s_add_u32 m0, s55, 98304\n"
        "s_add_u32 s80, s80, 32768\n"
        "s_sub_u32 s15, s80, 1409024\n"
        "s_min_u32 s80, s80, s15\n"
        "buffer_load_dwordx4 v22, s[20:23], s80 offen lds\n"
        "s_add_u32 m0, s55, 102400\n"
        "s_add_u32 s80, s80, 32768\n"
        "s_sub_u32 s15, s80, 1409024\n"
        "s_min_u32 s80, s80, s15\n"
        "buffer_load_dwordx4 v22, s[20:23], s80 offen lds\n"
        "s_add_u32 m0, s55, 106496\n"
        "s_add_u32 s80, s80, 32768\n"
        "s_sub_u32 s15, s80, 1409024\n"
        "s_min_u32 s80, s80, s15\n"
        "buffer_load_dwordx4 v22, s[20:23], s80 offen lds\n"
        "s_add_u32 m0, s55, 110592\n"
        "s_add_u32 s80, s80, 32768\n"
        "s_sub_u32 s15, s80, 1409024\n"
        "s_min_u32 s80, s80, s15\n"
        "buffer_load_dwordx4 v22, s[20:23], s80 offen lds\n"
        "s_add_u32 m0, s55, 114688\n"
        "s_add_u32 s80, s80, 32768\n"
        "s_sub_u32 s15, s80, 1409024\n"
        "s_min_u32 s80, s80, s15\n"
        "buffer_load_dwordx4 v22, s[20:23], s80 offen lds\n"
        "s_add_u32 m0, s55, 81920\n"
        "s_add_u32 s80, s80, 425984\n"
        "s_sub_u32 s15, s80, 1409024\n"
        "s_min_u32 s80, s80, s15\n"
        "buffer_load_dwordx4 v22, s[20:23], s80 offen lds\n"
        "v_mov_b32 v252, v145\n"
        "v_mov_b32 v253, v146\n"
        "v_mov_b32 v254, 0\n"
        "s_barrier\n"
        "s_add_u32 m0, s55, 86016\n"
        "s_add_u32 s80, s80, 32768\n"
        "s_sub_u32 s15, s80, 1409024\n"
        "s_min_u32 s80, s80, s15\n"
        "buffer_load_dwordx4 v22, s[20:23], s80 offen lds\n"
        "s_add_u32 m0, s55, 118784\n"
        "s_add_u32 s80, s80, 262144\n"
        "s_sub_u32 s15, s80, 1409024\n"
        "s_min_u32 s80, s80, s15\n"
        "buffer_load_dwordx4 v22, s[20:23], s80 offen lds\n"
        "s_add_u32 m0, s55, 122880\n"
        "s_add_u32 s80, s80, 32768\n"
        "s_sub_u32 s15, s80, 1409024\n"
        "s_min_u32 s80, s80, s15\n"
        "buffer_load_dwordx4 v22, s[20:23], s80 offen lds\n"
        "s_add_u32 m0, s55, 126976\n"
        "s_add_u32 s80, s80, 32768\n"
        "s_sub_u32 s15, s80, 1409024\n"
        "s_min_u32 s80, s80, s15\n"
        "buffer_load_dwordx4 v22, s[20:23], s80 offen lds\n"
        "s_add_u32 m0, s55, 131072\n"
        "s_add_u32 s80, s80, 32768\n"
        "s_sub_u32 s15, s80, 1409024\n"
        "s_min_u32 s80, s80, s15\n"
        "buffer_load_dwordx4 v22, s[20:23], s80 offen lds\n"
        "s_add_u32 m0, s55, 135168\n"
        "s_add_u32 s80, s80, 262144\n"
        "s_sub_u32 s15, s80, 1409024\n"
        "s_min_u32 s80, s80, s15\n"
        "buffer_load_dwordx4 v22, s[20:23], s80 offen lds\n"
        "s_waitcnt vmcnt(7)\n"
        "s_barrier\n"
        "s_add_u32 m0, s55, 139264\n"
        "s_add_u32 s80, s80, 32768\n"
        "s_sub_u32 s15, s80, 1409024\n"
        "s_min_u32 s80, s80, s15\n"
        "buffer_load_dwordx4 v22, s[20:23], s80 offen lds\n"
        "ds_read_b64_tr_b16 v[56:57], v8 offset:0\n"
        "ds_read_b64_tr_b16 v[58:59], v8 offset:512\n"
        "ds_read_b64_tr_b16 v[60:61], v9 offset:0\n"
        "ds_read_b64_tr_b16 v[62:63], v9 offset:512\n"
        "s_add_u32 m0, s55, 143360\n"
        "s_add_u32 s80, s80, 32768\n"
        "s_sub_u32 s15, s80, 1409024\n"
        "s_min_u32 s80, s80, s15\n"
        "buffer_load_dwordx4 v22, s[20:23], s80 offen lds\n"
        "ds_read_b64_tr_b16 v[64:65], v10 offset:0\n"
        "ds_read_b64_tr_b16 v[66:67], v10 offset:512\n"
        "ds_read_b64_tr_b16 v[68:69], v11 offset:0\n"
        "ds_read_b64_tr_b16 v[70:71], v11 offset:512\n"
        "ds_read_b64_tr_b16 v[72:73], v8 offset:4096\n"
        "ds_read_b64_tr_b16 v[74:75], v8 offset:4608\n"
        "ds_read_b64_tr_b16 v[76:77], v9 offset:4096\n"
        "ds_read_b64_tr_b16 v[78:79], v9 offset:4608\n"
        "ds_read_b64_tr_b16 v[80:81], v10 offset:4096\n"
        "ds_read_b64_tr_b16 v[82:83], v10 offset:4608\n"
        "ds_read_b64_tr_b16 v[84:85], v11 offset:4096\n"
        "ds_read_b64_tr_b16 v[86:87], v11 offset:4608\n"
        "ds_read_b64_tr_b16 v[88:89], v8 offset:8192\n"
        "ds_read_b64_tr_b16 v[90:91], v8 offset:8704\n"
        "ds_read_b64_tr_b16 v[92:93], v9 offset:8192\n"
        "ds_read_b64_tr_b16 v[94:95], v9 offset:8704\n"
        "ds_read_b64_tr_b16 v[96:97], v10 offset:8192\n"
        "ds_read_b64_tr_b16 v[98:99], v10 offset:8704\n"
        "ds_read_b64_tr_b16 v[100:101], v11 offset:8192\n"
        "ds_read_b64_tr_b16 v[102:103], v11 offset:8704\n"
        "ds_read_b64_tr_b16 v[104:105], v8 offset:12288\n"
        "ds_read_b64_tr_b16 v[106:107], v8 offset:12800\n"
        "ds_read_b64_tr_b16 v[108:109], v9 offset:12288\n"
        "ds_read_b64_tr_b16 v[110:111], v9 offset:12800\n"
        "ds_read_b64_tr_b16 v[112:113], v10 offset:12288\n"
        "ds_read_b64_tr_b16 v[114:115], v10 offset:12800\n"
        "ds_read_b64_tr_b16 v[116:117], v11 offset:12288\n"
        "ds_read_b64_tr_b16 v[118:119], v11 offset:12800\n"
        "ds_read_b64_tr_b16 v[120:121], v8 offset:16384\n"
        "ds_read_b64_tr_b16 v[122:123], v8 offset:16896\n"
        "ds_read_b64_tr_b16 v[124:125], v9 offset:16384\n"
        "ds_read_b64_tr_b16 v[126:127], v9 offset:16896\n"
        "ds_read_b64_tr_b16 v[128:129], v10 offset:16384\n"
        "ds_read_b64_tr_b16 v[130:131], v10 offset:16896\n"
        "ds_read_b64_tr_b16 v[132:133], v11 offset:16384\n"
        "ds_read_b64_tr_b16 v[134:135], v11 offset:16896\n"
        "ds_read_b64_tr_b16 v[136:137], v8 offset:20480\n"
        "ds_read_b64_tr_b16 v[138:139], v8 offset:20992\n"
        "ds_read_b64_tr_b16 v[140:141], v9 offset:20480\n"
        "ds_read_b64_tr_b16 v[142:143], v9 offset:20992\n"
        "ds_read_b64_tr_b16 v[144:145], v10 offset:20480\n"
        "ds_read_b64_tr_b16 v[146:147], v10 offset:20992\n"
        "ds_read_b64_tr_b16 v[148:149], v11 offset:20480\n"
        "ds_read_b64_tr_b16 v[150:151], v11 offset:20992\n"
        "ds_read_b64_tr_b16 v[152:153], v8 offset:24576\n"
        "ds_read_b64_tr_b16 v[154:155], v8 offset:25088\n"
        "ds_read_b64_tr_b16 v[156:157], v9 offset:24576\n"
        "ds_read_b64_tr_b16 v[158:159], v9 offset:25088\n"
        "ds_read_b64_tr_b16 v[160:161], v10 offset:24576\n"
        "ds_read_b64_tr_b16 v[162:163], v10 offset:25088\n"
        "ds_read_b64_tr_b16 v[164:165], v11 offset:24576\n"
        "ds_read_b64_tr_b16 v[166:167], v11 offset:25088\n"
        "s_cmp_eq_u32 s82, 0\n"
        "s_cselect_b32 s76, 15, 8\n"
        "s_lshl_b32 s76, s76, 12\n"
        "ds_read_b128 v[200:203], v18 offset:0\n"
        "ds_read_b128 v[204:207], v18 offset:512\n"
        "ds_read_b128 v[208:211], v18 offset:1024\n"
        "ds_read_b128 v[212:215], v18 offset:1536\n"
        "ds_read_b128 v[216:219], v18 offset:2048\n"
        "ds_read_b128 v[220:223], v18 offset:2560\n"
        "ds_read_b128 v[224:227], v18 offset:3072\n"
        "ds_read_b128 v[228:231], v18 offset:3584\n"
        "v_add_u32 v8, s76, v8\n"
        "v_add_u32 v9, s76, v9\n"
        "v_add_u32 v10, s76, v10\n"
        "v_add_u32 v11, s76, v11\n"
        "s_lshl2_add_u32 s76, s82, 0\n"
        "v_mov_b32 v16, s76\n"
        "s_add_u32 s96, s81, 4096\n"
        "s_waitcnt vmcnt(0)\n"
        "ds_write_b32 v253, v254 offset:4160\n"
        "ds_write_b128 v252, v[168:171] offset:64\n"
        "ds_write_b128 v252, v[172:175] offset:4224\n"
        "ds_write_b128 v252, v[176:179] offset:8384\n"
        "ds_write_b128 v252, v[180:183] offset:12544\n"
        "s_or_b32 s75, s3, s46\n"
        "s_cbranch_scc1 .Lep_24_%=\n"
        "s_mov_b64 exec, 1\n"
        "buffer_store_dwordx2 v[6:7], off, s[16:19], s75 sc1\n"
        "s_mov_b64 exec, -1\n"
        ".Lep_24_%=:\n"
        "s_waitcnt lgkmcnt(0)\n"
        "s_barrier\n"
        "ds_read2_b32 v[168:169], v251 offset0:128 offset1:129\n"
        "ds_read2_b32 v[170:171], v251 offset0:130 offset1:131\n"
        "ds_read2_b32 v[172:173], v251 offset0:0 offset1:1\n"
        "ds_read2_b32 v[174:175], v251 offset0:2 offset1:3\n"
        "ds_read2_b32 v[176:177], v250 offset0:128 offset1:129\n"
        "ds_read2_b32 v[178:179], v250 offset0:130 offset1:131\n"
        "ds_read2_b32 v[180:181], v250 offset0:0 offset1:1\n"
        "ds_read2_b32 v[182:183], v250 offset0:2 offset1:3\n"
        "ds_read2_b32 v[184:185], v249 offset0:128 offset1:129\n"
        "ds_read2_b32 v[186:187], v249 offset0:130 offset1:131\n"
        "ds_read2_b32 v[188:189], v249 offset0:0 offset1:1\n"
        "ds_read2_b32 v[190:191], v249 offset0:2 offset1:3\n"
        "ds_read2_b32 v[192:193], v248 offset0:128 offset1:129\n"
        "ds_read2_b32 v[194:195], v248 offset0:130 offset1:131\n"
        "ds_read2_b32 v[196:197], v248 offset0:0 offset1:1\n"
        "ds_read2_b32 v[198:199], v248 offset0:2 offset1:3\n"
        "s_waitcnt lgkmcnt(14)\n"
        "v_mfma_f32_16x16x32_bf16 v[52:55], v[168:171], v[228:231], 0\n"
        "ds_read_b128 v[228:231], v18 offset:3648\n"
        "s_waitcnt lgkmcnt(15)\n"
        "v_mfma_f32_16x16x32_bf16 v[48:51], v[168:171], v[224:227], 0\n"
        "s_waitcnt lgkmcnt(15)\n"
        "v_mfma_f32_16x16x32_bf16 v[44:47], v[168:171], v[220:223], 0\n"
        "s_waitcnt lgkmcnt(15)\n"
        "v_mfma_f32_16x16x32_bf16 v[40:43], v[168:171], v[216:219], 0\n"
        "s_waitcnt lgkmcnt(15)\n"
        "v_mfma_f32_16x16x32_bf16 v[36:39], v[168:171], v[212:215], 0\n"
        "s_waitcnt lgkmcnt(15)\n"
        "v_mfma_f32_16x16x32_bf16 v[32:35], v[168:171], v[208:211], 0\n"
        "s_waitcnt lgkmcnt(15)\n"
        "v_mfma_f32_16x16x32_bf16 v[28:31], v[168:171], v[204:207], 0\n"
        "s_waitcnt lgkmcnt(15)\n"
        "v_mfma_f32_16x16x32_bf16 v[24:27], v[168:171], v[200:203], 0\n"
        "ds_read2_b32 v[168:169], v251 offset0:144 offset1:145\n"
        "s_waitcnt lgkmcnt(14)\n"
        "v_mfma_f32_16x16x32_bf16 v[52:55], v[172:175], v[224:227], v[52:55]\n"
        "ds_read_b128 v[224:227], v18 offset:3136\n"
        "v_mfma_f32_16x16x32_bf16 v[48:51], v[172:175], v[220:223], v[48:51]\n"
        "ds_read2_b32 v[170:171], v251 offset0:146 offset1:147\n"
        "v_mfma_f32_16x16x32_bf16 v[44:47], v[172:175], v[216:219], v[44:47]\n"
        "v_mfma_f32_16x16x32_bf16 v[40:43], v[172:175], v[212:215], v[40:43]\n"
        "v_mfma_f32_16x16x32_bf16 v[36:39], v[172:175], v[208:211], v[36:39]\n"
        "v_mfma_f32_16x16x32_bf16 v[32:35], v[172:175], v[204:207], v[32:35]\n"
        "v_mfma_f32_16x16x32_bf16 v[28:31], v[172:175], v[200:203], v[28:31]\n"
        "ds_read2_b32 v[172:173], v251 offset0:16 offset1:17\n"
        "s_waitcnt lgkmcnt(15)\n"
        "v_mfma_f32_16x16x32_bf16 v[52:55], v[176:179], v[220:223], v[52:55]\n"
        "ds_read_b128 v[220:223], v18 offset:2624\n"
        "v_mfma_f32_16x16x32_bf16 v[48:51], v[176:179], v[216:219], v[48:51]\n"
        "ds_read2_b32 v[174:175], v251 offset0:18 offset1:19\n"
        "v_mfma_f32_16x16x32_bf16 v[44:47], v[176:179], v[212:215], v[44:47]\n"
        "v_mfma_f32_16x16x32_bf16 v[40:43], v[176:179], v[208:211], v[40:43]\n"
        "v_mfma_f32_16x16x32_bf16 v[36:39], v[176:179], v[204:207], v[36:39]\n"
        "v_mfma_f32_16x16x32_bf16 v[32:35], v[176:179], v[200:203], v[32:35]\n"
        "ds_read2_b32 v[176:177], v250 offset0:144 offset1:145\n"
        "s_waitcnt lgkmcnt(15)\n"
        "v_mfma_f32_16x16x32_bf16 v[52:55], v[180:183], v[216:219], v[52:55]\n"
        "ds_read_b128 v[216:219], v18 offset:2112\n"
        "v_mfma_f32_16x16x32_bf16 v[48:51], v[180:183], v[212:215], v[48:51]\n"
        "ds_read2_b32 v[178:179], v250 offset0:146 offset1:147\n"
        "v_mfma_f32_16x16x32_bf16 v[44:47], v[180:183], v[208:211], v[44:47]\n"
        "v_mfma_f32_16x16x32_bf16 v[40:43], v[180:183], v[204:207], v[40:43]\n"
        "v_mfma_f32_16x16x32_bf16 v[36:39], v[180:183], v[200:203], v[36:39]\n"
        "ds_read2_b32 v[180:181], v250 offset0:16 offset1:17\n"
        "s_waitcnt lgkmcnt(15)\n"
        "v_mfma_f32_16x16x32_bf16 v[52:55], v[184:187], v[212:215], v[52:55]\n"
        "ds_read_b128 v[212:215], v18 offset:1600\n"
        "v_mfma_f32_16x16x32_bf16 v[48:51], v[184:187], v[208:211], v[48:51]\n"
        "ds_read2_b32 v[182:183], v250 offset0:18 offset1:19\n"
        "v_mfma_f32_16x16x32_bf16 v[44:47], v[184:187], v[204:207], v[44:47]\n"
        "v_mfma_f32_16x16x32_bf16 v[40:43], v[184:187], v[200:203], v[40:43]\n"
        "ds_read2_b32 v[184:185], v249 offset0:144 offset1:145\n"
        "s_waitcnt lgkmcnt(15)\n"
        "v_mfma_f32_16x16x32_bf16 v[52:55], v[188:191], v[208:211], v[52:55]\n"
        "ds_read_b128 v[208:211], v18 offset:1088\n"
        "v_mfma_f32_16x16x32_bf16 v[48:51], v[188:191], v[204:207], v[48:51]\n"
        "ds_read2_b32 v[186:187], v249 offset0:146 offset1:147\n"
        "v_mfma_f32_16x16x32_bf16 v[44:47], v[188:191], v[200:203], v[44:47]\n"
        "ds_read2_b32 v[188:189], v249 offset0:16 offset1:17\n"
        "s_waitcnt lgkmcnt(15)\n"
        "v_mfma_f32_16x16x32_bf16 v[52:55], v[192:195], v[204:207], v[52:55]\n"
        "ds_read_b128 v[204:207], v18 offset:576\n"
        "v_mfma_f32_16x16x32_bf16 v[48:51], v[192:195], v[200:203], v[48:51]\n"
        "ds_read2_b32 v[190:191], v249 offset0:18 offset1:19\n"
        "s_waitcnt lgkmcnt(15)\n"
        "v_mfma_f32_16x16x32_bf16 v[52:55], v[196:199], v[200:203], v[52:55]\n"
        "ds_read_b128 v[200:203], v18 offset:64\n"
        "s_waitcnt lgkmcnt(15)\n"
        "v_mfma_f32_16x16x32_bf16 v[52:55], v[168:171], v[228:231], v[52:55]\n"
        "ds_read_b128 v[228:231], v18 offset:3712\n"
        "s_waitcnt lgkmcnt(15)\n"
        "v_mfma_f32_16x16x32_bf16 v[48:51], v[168:171], v[224:227], v[48:51]\n"
        "ds_read2_b32 v[192:193], v248 offset0:144 offset1:145\n"
        "s_waitcnt lgkmcnt(15)\n"
        "v_mfma_f32_16x16x32_bf16 v[44:47], v[168:171], v[220:223], v[44:47]\n"
        "ds_read2_b32 v[194:195], v248 offset0:146 offset1:147\n"
        "s_waitcnt lgkmcnt(14)\n"
        "v_mfma_f32_16x16x32_bf16 v[40:43], v[168:171], v[216:219], v[40:43]\n"
        "ds_read2_b32 v[196:197], v248 offset0:16 offset1:17\n"
        "s_waitcnt lgkmcnt(12)\n"
        "v_mfma_f32_16x16x32_bf16 v[36:39], v[168:171], v[212:215], v[36:39]\n"
        "ds_read2_b32 v[198:199], v248 offset0:18 offset1:19\n"
        "s_waitcnt lgkmcnt(10)\n"
        "v_mfma_f32_16x16x32_bf16 v[32:35], v[168:171], v[208:211], v[32:35]\n"
        "s_waitcnt lgkmcnt(7)\n"
        "v_mfma_f32_16x16x32_bf16 v[28:31], v[168:171], v[204:207], v[28:31]\n"
        "s_waitcnt lgkmcnt(5)\n"
        "v_mfma_f32_16x16x32_bf16 v[24:27], v[168:171], v[200:203], v[24:27]\n"
        "ds_read2_b32 v[168:169], v251 offset0:160 offset1:161\n"
        "s_waitcnt lgkmcnt(15)\n"
        "v_mfma_f32_16x16x32_bf16 v[52:55], v[172:175], v[224:227], v[52:55]\n"
        "ds_read_b128 v[224:227], v18 offset:3200\n"
        "v_mfma_f32_16x16x32_bf16 v[48:51], v[172:175], v[220:223], v[48:51]\n"
        "ds_read2_b32 v[170:171], v251 offset0:162 offset1:163\n"
        "v_mfma_f32_16x16x32_bf16 v[44:47], v[172:175], v[216:219], v[44:47]\n"
        "v_mfma_f32_16x16x32_bf16 v[40:43], v[172:175], v[212:215], v[40:43]\n"
        "v_mfma_f32_16x16x32_bf16 v[36:39], v[172:175], v[208:211], v[36:39]\n"
        "v_mfma_f32_16x16x32_bf16 v[32:35], v[172:175], v[204:207], v[32:35]\n"
        "v_mfma_f32_16x16x32_bf16 v[28:31], v[172:175], v[200:203], v[28:31]\n"
        "ds_read2_b32 v[172:173], v251 offset0:32 offset1:33\n"
        "s_waitcnt lgkmcnt(15)\n"
        "v_mfma_f32_16x16x32_bf16 v[52:55], v[176:179], v[220:223], v[52:55]\n"
        "ds_read_b128 v[220:223], v18 offset:2688\n"
        "v_mfma_f32_16x16x32_bf16 v[48:51], v[176:179], v[216:219], v[48:51]\n"
        "ds_read2_b32 v[174:175], v251 offset0:34 offset1:35\n"
        "v_mfma_f32_16x16x32_bf16 v[44:47], v[176:179], v[212:215], v[44:47]\n"
        "v_mfma_f32_16x16x32_bf16 v[40:43], v[176:179], v[208:211], v[40:43]\n"
        "v_mfma_f32_16x16x32_bf16 v[36:39], v[176:179], v[204:207], v[36:39]\n"
        "v_mfma_f32_16x16x32_bf16 v[32:35], v[176:179], v[200:203], v[32:35]\n"
        "ds_read2_b32 v[176:177], v250 offset0:160 offset1:161\n"
        "s_waitcnt lgkmcnt(15)\n"
        "v_mfma_f32_16x16x32_bf16 v[52:55], v[180:183], v[216:219], v[52:55]\n"
        "ds_read_b128 v[216:219], v18 offset:2176\n"
        "v_mfma_f32_16x16x32_bf16 v[48:51], v[180:183], v[212:215], v[48:51]\n"
        "ds_read2_b32 v[178:179], v250 offset0:162 offset1:163\n"
        "v_mfma_f32_16x16x32_bf16 v[44:47], v[180:183], v[208:211], v[44:47]\n"
        "v_mfma_f32_16x16x32_bf16 v[40:43], v[180:183], v[204:207], v[40:43]\n"
        "v_mfma_f32_16x16x32_bf16 v[36:39], v[180:183], v[200:203], v[36:39]\n"
        "ds_read2_b32 v[180:181], v250 offset0:32 offset1:33\n"
        "s_waitcnt lgkmcnt(15)\n"
        "v_mfma_f32_16x16x32_bf16 v[52:55], v[184:187], v[212:215], v[52:55]\n"
        "ds_read_b128 v[212:215], v18 offset:1664\n"
        "v_mfma_f32_16x16x32_bf16 v[48:51], v[184:187], v[208:211], v[48:51]\n"
        "ds_read2_b32 v[182:183], v250 offset0:34 offset1:35\n"
        "v_mfma_f32_16x16x32_bf16 v[44:47], v[184:187], v[204:207], v[44:47]\n"
        "v_mfma_f32_16x16x32_bf16 v[40:43], v[184:187], v[200:203], v[40:43]\n"
        "ds_read2_b32 v[184:185], v249 offset0:160 offset1:161\n"
        "s_waitcnt lgkmcnt(15)\n"
        "v_mfma_f32_16x16x32_bf16 v[52:55], v[188:191], v[208:211], v[52:55]\n"
        "ds_read_b128 v[208:211], v18 offset:1152\n"
        "v_mfma_f32_16x16x32_bf16 v[48:51], v[188:191], v[204:207], v[48:51]\n"
        "ds_read2_b32 v[186:187], v249 offset0:162 offset1:163\n"
        "v_mfma_f32_16x16x32_bf16 v[44:47], v[188:191], v[200:203], v[44:47]\n"
        "ds_read2_b32 v[188:189], v249 offset0:32 offset1:33\n"
        "s_waitcnt lgkmcnt(15)\n"
        "v_mfma_f32_16x16x32_bf16 v[52:55], v[192:195], v[204:207], v[52:55]\n"
        "ds_read_b128 v[204:207], v18 offset:640\n"
        "v_mfma_f32_16x16x32_bf16 v[48:51], v[192:195], v[200:203], v[48:51]\n"
        "ds_read2_b32 v[190:191], v249 offset0:34 offset1:35\n"
        "s_waitcnt lgkmcnt(15)\n"
        "v_mfma_f32_16x16x32_bf16 v[52:55], v[196:199], v[200:203], v[52:55]\n"
        "ds_read_b128 v[200:203], v18 offset:128\n"
        "s_waitcnt lgkmcnt(15)\n"
        "v_mfma_f32_16x16x32_bf16 v[52:55], v[168:171], v[228:231], v[52:55]\n"
        "ds_read_b128 v[228:231], v18 offset:3776\n"
        "s_waitcnt lgkmcnt(15)\n"
        "v_mfma_f32_16x16x32_bf16 v[48:51], v[168:171], v[224:227], v[48:51]\n"
        "ds_read2_b32 v[192:193], v248 offset0:160 offset1:161\n"
        "s_waitcnt lgkmcnt(15)\n"
        "v_mfma_f32_16x16x32_bf16 v[44:47], v[168:171], v[220:223], v[44:47]\n"
        "ds_read2_b32 v[194:195], v248 offset0:162 offset1:163\n"
        "s_waitcnt lgkmcnt(14)\n"
        "v_mfma_f32_16x16x32_bf16 v[40:43], v[168:171], v[216:219], v[40:43]\n"
        "ds_read2_b32 v[196:197], v248 offset0:32 offset1:33\n"
        "s_waitcnt lgkmcnt(12)\n"
        "v_mfma_f32_16x16x32_bf16 v[36:39], v[168:171], v[212:215], v[36:39]\n"
        "ds_read2_b32 v[198:199], v248 offset0:34 offset1:35\n"
        "s_waitcnt lgkmcnt(10)\n"
        "v_mfma_f32_16x16x32_bf16 v[32:35], v[168:171], v[208:211], v[32:35]\n"
        "s_waitcnt lgkmcnt(7)\n"
        "v_mfma_f32_16x16x32_bf16 v[28:31], v[168:171], v[204:207], v[28:31]\n"
        "s_waitcnt lgkmcnt(5)\n"
        "v_mfma_f32_16x16x32_bf16 v[24:27], v[168:171], v[200:203], v[24:27]\n"
        "ds_read2_b32 v[168:169], v251 offset0:176 offset1:177\n"
        "s_waitcnt lgkmcnt(15)\n"
        "v_mfma_f32_16x16x32_bf16 v[52:55], v[172:175], v[224:227], v[52:55]\n"
        "ds_read_b128 v[224:227], v18 offset:3264\n"
        "v_mfma_f32_16x16x32_bf16 v[48:51], v[172:175], v[220:223], v[48:51]\n"
        "ds_read2_b32 v[170:171], v251 offset0:178 offset1:179\n"
        "v_mfma_f32_16x16x32_bf16 v[44:47], v[172:175], v[216:219], v[44:47]\n"
        "v_mfma_f32_16x16x32_bf16 v[40:43], v[172:175], v[212:215], v[40:43]\n"
        "v_mfma_f32_16x16x32_bf16 v[36:39], v[172:175], v[208:211], v[36:39]\n"
        "v_mfma_f32_16x16x32_bf16 v[32:35], v[172:175], v[204:207], v[32:35]\n"
        "v_mfma_f32_16x16x32_bf16 v[28:31], v[172:175], v[200:203], v[28:31]\n"
        "ds_read2_b32 v[172:173], v251 offset0:48 offset1:49\n"
        "s_waitcnt lgkmcnt(15)\n"
        "v_mfma_f32_16x16x32_bf16 v[52:55], v[176:179], v[220:223], v[52:55]\n"
        "ds_read_b128 v[220:223], v18 offset:2752\n"
        "v_mfma_f32_16x16x32_bf16 v[48:51], v[176:179], v[216:219], v[48:51]\n"
        "ds_read2_b32 v[174:175], v251 offset0:50 offset1:51\n"
        "v_mfma_f32_16x16x32_bf16 v[44:47], v[176:179], v[212:215], v[44:47]\n"
        "v_mfma_f32_16x16x32_bf16 v[40:43], v[176:179], v[208:211], v[40:43]\n"
        "v_mfma_f32_16x16x32_bf16 v[36:39], v[176:179], v[204:207], v[36:39]\n"
        "v_mfma_f32_16x16x32_bf16 v[32:35], v[176:179], v[200:203], v[32:35]\n"
        "ds_read2_b32 v[176:177], v250 offset0:176 offset1:177\n"
        "s_waitcnt lgkmcnt(15)\n"
        "v_mfma_f32_16x16x32_bf16 v[52:55], v[180:183], v[216:219], v[52:55]\n"
        "ds_read_b128 v[216:219], v18 offset:2240\n"
        "v_mfma_f32_16x16x32_bf16 v[48:51], v[180:183], v[212:215], v[48:51]\n"
        "ds_read2_b32 v[178:179], v250 offset0:178 offset1:179\n"
        "v_mfma_f32_16x16x32_bf16 v[44:47], v[180:183], v[208:211], v[44:47]\n"
        "v_mfma_f32_16x16x32_bf16 v[40:43], v[180:183], v[204:207], v[40:43]\n"
        "v_mfma_f32_16x16x32_bf16 v[36:39], v[180:183], v[200:203], v[36:39]\n"
        "ds_read2_b32 v[180:181], v250 offset0:48 offset1:49\n"
        "s_waitcnt lgkmcnt(15)\n"
        "v_mfma_f32_16x16x32_bf16 v[52:55], v[184:187], v[212:215], v[52:55]\n"
        "ds_read_b128 v[212:215], v18 offset:1728\n"
        "v_mfma_f32_16x16x32_bf16 v[48:51], v[184:187], v[208:211], v[48:51]\n"
        "ds_read2_b32 v[182:183], v250 offset0:50 offset1:51\n"
        "v_mfma_f32_16x16x32_bf16 v[44:47], v[184:187], v[204:207], v[44:47]\n"
        "v_mfma_f32_16x16x32_bf16 v[40:43], v[184:187], v[200:203], v[40:43]\n"
        "ds_read2_b32 v[184:185], v249 offset0:176 offset1:177\n"
        "s_waitcnt lgkmcnt(15)\n"
        "v_mfma_f32_16x16x32_bf16 v[52:55], v[188:191], v[208:211], v[52:55]\n"
        "ds_read_b128 v[208:211], v18 offset:1216\n"
        "v_mfma_f32_16x16x32_bf16 v[48:51], v[188:191], v[204:207], v[48:51]\n"
        "ds_read2_b32 v[186:187], v249 offset0:178 offset1:179\n"
        "s_mov_b64 exec, s[38:39]\n"
        "buffer_load_dwordx2 v[14:15], v255, s[16:19], s32 offen sc1\n"
        "s_mov_b64 exec, -1\n"
        "v_mfma_f32_16x16x32_bf16 v[44:47], v[188:191], v[200:203], v[44:47]\n"
        "ds_read2_b32 v[188:189], v249 offset0:48 offset1:49\n"
        "s_waitcnt lgkmcnt(15)\n"
        "v_mfma_f32_16x16x32_bf16 v[52:55], v[192:195], v[204:207], v[52:55]\n"
        "ds_read_b128 v[204:207], v18 offset:704\n"
        "v_mfma_f32_16x16x32_bf16 v[48:51], v[192:195], v[200:203], v[48:51]\n"
        "ds_read2_b32 v[190:191], v249 offset0:50 offset1:51\n"
        "s_waitcnt lgkmcnt(15)\n"
        "v_mfma_f32_16x16x32_bf16 v[52:55], v[196:199], v[200:203], v[52:55]\n"
        "ds_read_b128 v[200:203], v18 offset:192\n"
        "ds_read2_b32 v[192:193], v248 offset0:176 offset1:177\n"
        "ds_read2_b32 v[194:195], v248 offset0:178 offset1:179\n"
        "ds_read2_b32 v[196:197], v248 offset0:48 offset1:49\n"
        "ds_read2_b32 v[198:199], v248 offset0:50 offset1:51\n"
        "s_cmp_eq_u32 s83, 0\n"
        "s_cbranch_scc0 .Ly_25_%=\n"
        "s_waitcnt lgkmcnt(15)\n"
        "v_mfma_f32_16x16x32_bf16 v[44:47], v[168:171], v[220:223], v[44:47]\n"
        "s_waitcnt lgkmcnt(15)\n"
        "v_mfma_f32_16x16x32_bf16 v[44:47], v[172:175], v[216:219], v[44:47]\n"
        "s_waitcnt lgkmcnt(12)\n"
        "v_mfma_f32_16x16x32_bf16 v[44:47], v[176:179], v[212:215], v[44:47]\n"
        "s_waitcnt lgkmcnt(9)\n"
        "v_mfma_f32_16x16x32_bf16 v[44:47], v[180:183], v[208:211], v[44:47]\n"
        "s_waitcnt lgkmcnt(6)\n"
        "v_mfma_f32_16x16x32_bf16 v[44:47], v[184:187], v[204:207], v[44:47]\n"
        "s_waitcnt lgkmcnt(4)\n"
        "v_mfma_f32_16x16x32_bf16 v[44:47], v[188:191], v[200:203], v[44:47]\n"
        "v_mfma_f32_16x16x32_bf16 v[40:43], v[168:171], v[216:219], v[40:43]\n"
        "v_mfma_f32_16x16x32_bf16 v[40:43], v[172:175], v[212:215], v[40:43]\n"
        "v_mfma_f32_16x16x32_bf16 v[40:43], v[176:179], v[208:211], v[40:43]\n"
        "v_mfma_f32_16x16x32_bf16 v[40:43], v[180:183], v[204:207], v[40:43]\n"
        "v_mfma_f32_16x16x32_bf16 v[40:43], v[184:187], v[200:203], v[40:43]\n"
        "v_mfma_f32_16x16x32_bf16 v[36:39], v[168:171], v[212:215], v[36:39]\n"
        "v_mfma_f32_16x16x32_bf16 v[36:39], v[172:175], v[208:211], v[36:39]\n"
        "v_mfma_f32_16x16x32_bf16 v[36:39], v[176:179], v[204:207], v[36:39]\n"
        "ds_write_b128 v20, v[44:47] offset:25856\n"
        "v_mfma_f32_16x16x32_bf16 v[36:39], v[180:183], v[200:203], v[36:39]\n"
        "v_mfma_f32_16x16x32_bf16 v[32:35], v[168:171], v[208:211], v[32:35]\n"
        "v_mfma_f32_16x16x32_bf16 v[32:35], v[172:175], v[204:207], v[32:35]\n"
        "v_mfma_f32_16x16x32_bf16 v[32:35], v[176:179], v[200:203], v[32:35]\n"
        "s_waitcnt lgkmcnt(15)\n"
        "v_mfma_f32_16x16x32_bf16 v[52:55], v[168:171], v[228:231], v[52:55]\n"
        "ds_write_b128 v20, v[40:43] offset:26880\n"
        "s_waitcnt lgkmcnt(15)\n"
        "v_mfma_f32_16x16x32_bf16 v[52:55], v[172:175], v[224:227], v[52:55]\n"
        "v_mfma_f32_16x16x32_bf16 v[52:55], v[176:179], v[220:223], v[52:55]\n"
        "v_mfma_f32_16x16x32_bf16 v[52:55], v[180:183], v[216:219], v[52:55]\n"
        "v_mfma_f32_16x16x32_bf16 v[52:55], v[184:187], v[212:215], v[52:55]\n"
        "ds_write_b128 v20, v[36:39] offset:27904\n"
        "v_mfma_f32_16x16x32_bf16 v[52:55], v[188:191], v[208:211], v[52:55]\n"
        "s_waitcnt lgkmcnt(5)\n"
        "v_mfma_f32_16x16x32_bf16 v[52:55], v[192:195], v[204:207], v[52:55]\n"
        "s_waitcnt lgkmcnt(3)\n"
        "v_mfma_f32_16x16x32_bf16 v[52:55], v[196:199], v[200:203], v[52:55]\n"
        "ds_write_b128 v20, v[32:35] offset:28928\n"
        "s_mov_b64 exec, 1\n"
        "ds_add_u32 v16, v5 offset:12\n"
        "s_mov_b64 exec, -1\n"
        "v_mfma_f32_16x16x32_bf16 v[48:51], v[168:171], v[224:227], v[48:51]\n"
        "v_mfma_f32_16x16x32_bf16 v[48:51], v[172:175], v[220:223], v[48:51]\n"
        "v_mfma_f32_16x16x32_bf16 v[48:51], v[176:179], v[216:219], v[48:51]\n"
        "v_mfma_f32_16x16x32_bf16 v[48:51], v[180:183], v[212:215], v[48:51]\n"
        "v_mfma_f32_16x16x32_bf16 v[48:51], v[184:187], v[208:211], v[48:51]\n"
        "v_mfma_f32_16x16x32_bf16 v[48:51], v[188:191], v[204:207], v[48:51]\n"
        "v_mfma_f32_16x16x32_bf16 v[48:51], v[192:195], v[200:203], v[48:51]\n"
        "v_mfma_f32_16x16x32_bf16 v[28:31], v[168:171], v[204:207], v[28:31]\n"
        "v_mfma_f32_16x16x32_bf16 v[28:31], v[172:175], v[200:203], v[28:31]\n"
        "v_mfma_f32_16x16x32_bf16 v[24:27], v[168:171], v[200:203], v[24:27]\n"
        "s_setprio 3\n"
        "s_waitcnt vmcnt(0)\n"
        "v_cmp_ne_u64 s[42:43], s[62:63], v[14:15]\n"
        "s_and_b32 s33, s42, s34\n"
        "s_cbranch_scc1 .Lsl_27_%=\n"
        "buffer_load_dwordx4 v[192:195], v12, s[16:19], s64 offen sc1\n"
        "buffer_load_dwordx4 v[196:199], v12, s[16:19], s64 offen offset:256 sc1\n"
        "buffer_load_dwordx4 v[200:203], v12, s[16:19], s65 offen sc1\n"
        "buffer_load_dwordx4 v[204:207], v12, s[16:19], s65 offen offset:256 sc1\n"
        "buffer_load_dwordx4 v[208:211], v12, s[16:19], s66 offen sc1\n"
        "buffer_load_dwordx4 v[212:215], v12, s[16:19], s66 offen offset:256 sc1\n"
        "s_branch .Ljn_28_%=\n"
        ".Lsl_27_%=:\n"
        "s_and_b64 exec, s[42:43], s[38:39]\n"
        "buffer_load_dwordx2 v[14:15], v255, s[16:19], s32 offen sc1\n"
        "s_mov_b64 exec, -1\n"
        ".Ljn_28_%=:\n"
        "s_add_u32 s40, s81, 6144\n"
        "s_add_u32 s41, s81, 0\n"
        ".Llw_29_%=:\n"
        "ds_read_b32 v13, v16 offset:12\n"
        "s_waitcnt lgkmcnt(0)\n"
        "v_readfirstlane_b32 s75, v13\n"
        "s_cmp_ge_u32 s75, 2\n"
        "s_cbranch_scc1 .Llwd_30_%=\n"
        "s_add_u32 s61, s61, 1\n"
        "s_cmp_lt_u32 s61, 0x100000\n"
        "s_cbranch_scc1 .Llw_29_%=\n"
        ".Llwd_30_%=:\n"
        "ds_read_b128 v[172:175], v21 offset:25856\n"
        "ds_read_b128 v[176:179], v21 offset:26880\n"
        "ds_read_b128 v[180:183], v21 offset:27904\n"
        "ds_read_b128 v[184:187], v21 offset:28928\n"
        "s_mov_b64 exec, 1\n"
        "ds_add_u32 v4, v5 offset:4\n"
        "s_mov_b64 exec, -1\n"
        "s_waitcnt lgkmcnt(4)\n"
        "v_add_f32 v52, v52, v172\n"
        "v_add_f32 v53, v53, v173\n"
        "v_add_f32 v54, v54, v174\n"
        "v_add_f32 v55, v55, v175\n"
        "v_cvt_pk_bf16_f32 v232, v52, v53\n"
        "v_cvt_pk_bf16_f32 v234, v54, v55\n"
        "s_waitcnt lgkmcnt(3)\n"
        "v_add_f32 v48, v48, v176\n"
        "v_add_f32 v49, v49, v177\n"
        "v_add_f32 v50, v50, v178\n"
        "v_add_f32 v51, v51, v179\n"
        "v_cvt_pk_bf16_f32 v236, v48, v49\n"
        "v_cvt_pk_bf16_f32 v238, v50, v51\n"
        "s_waitcnt lgkmcnt(2)\n"
        "v_add_f32 v28, v28, v180\n"
        "v_add_f32 v29, v29, v181\n"
        "v_add_f32 v30, v30, v182\n"
        "v_add_f32 v31, v31, v183\n"
        "v_cvt_pk_bf16_f32 v240, v28, v29\n"
        "v_cvt_pk_bf16_f32 v242, v30, v31\n"
        "s_waitcnt lgkmcnt(1)\n"
        "v_add_f32 v24, v24, v184\n"
        "v_add_f32 v25, v25, v185\n"
        "v_add_f32 v26, v26, v186\n"
        "v_add_f32 v27, v27, v187\n"
        "v_cvt_pk_bf16_f32 v244, v24, v25\n"
        "v_cvt_pk_bf16_f32 v246, v26, v27\n"
        "s_branch .Ly_26_%=\n"
        ".Ly_25_%=:\n"
        "s_waitcnt lgkmcnt(15)\n"
        "v_mfma_f32_16x16x32_bf16 v[52:55], v[168:171], v[228:231], v[52:55]\n"
        "s_waitcnt lgkmcnt(15)\n"
        "v_mfma_f32_16x16x32_bf16 v[52:55], v[172:175], v[224:227], v[52:55]\n"
        "s_waitcnt lgkmcnt(14)\n"
        "v_mfma_f32_16x16x32_bf16 v[52:55], v[176:179], v[220:223], v[52:55]\n"
        "s_waitcnt lgkmcnt(11)\n"
        "v_mfma_f32_16x16x32_bf16 v[52:55], v[180:183], v[216:219], v[52:55]\n"
        "s_waitcnt lgkmcnt(8)\n"
        "v_mfma_f32_16x16x32_bf16 v[52:55], v[184:187], v[212:215], v[52:55]\n"
        "s_waitcnt lgkmcnt(5)\n"
        "v_mfma_f32_16x16x32_bf16 v[52:55], v[188:191], v[208:211], v[52:55]\n"
        "s_waitcnt lgkmcnt(2)\n"
        "v_mfma_f32_16x16x32_bf16 v[52:55], v[192:195], v[204:207], v[52:55]\n"
        "s_waitcnt lgkmcnt(0)\n"
        "v_mfma_f32_16x16x32_bf16 v[52:55], v[196:199], v[200:203], v[52:55]\n"
        "v_mfma_f32_16x16x32_bf16 v[48:51], v[168:171], v[224:227], v[48:51]\n"
        "v_mfma_f32_16x16x32_bf16 v[48:51], v[172:175], v[220:223], v[48:51]\n"
        "v_mfma_f32_16x16x32_bf16 v[48:51], v[176:179], v[216:219], v[48:51]\n"
        "v_mfma_f32_16x16x32_bf16 v[48:51], v[180:183], v[212:215], v[48:51]\n"
        "v_mfma_f32_16x16x32_bf16 v[48:51], v[184:187], v[208:211], v[48:51]\n"
        "v_mfma_f32_16x16x32_bf16 v[48:51], v[188:191], v[204:207], v[48:51]\n"
        "v_mfma_f32_16x16x32_bf16 v[48:51], v[192:195], v[200:203], v[48:51]\n"
        "v_mfma_f32_16x16x32_bf16 v[28:31], v[168:171], v[204:207], v[28:31]\n"
        "ds_write_b128 v20, v[52:55] offset:25856\n"
        "v_mfma_f32_16x16x32_bf16 v[28:31], v[172:175], v[200:203], v[28:31]\n"
        "v_mfma_f32_16x16x32_bf16 v[24:27], v[168:171], v[200:203], v[24:27]\n"
        "v_mfma_f32_16x16x32_bf16 v[44:47], v[168:171], v[220:223], v[44:47]\n"
        "v_mfma_f32_16x16x32_bf16 v[44:47], v[172:175], v[216:219], v[44:47]\n"
        "v_mfma_f32_16x16x32_bf16 v[44:47], v[176:179], v[212:215], v[44:47]\n"
        "v_mfma_f32_16x16x32_bf16 v[44:47], v[180:183], v[208:211], v[44:47]\n"
        "v_mfma_f32_16x16x32_bf16 v[44:47], v[184:187], v[204:207], v[44:47]\n"
        "ds_write_b128 v20, v[48:51] offset:26880\n"
        "v_mfma_f32_16x16x32_bf16 v[44:47], v[188:191], v[200:203], v[44:47]\n"
        "v_mfma_f32_16x16x32_bf16 v[40:43], v[168:171], v[216:219], v[40:43]\n"
        "ds_write_b128 v20, v[28:31] offset:27904\n"
        "v_mfma_f32_16x16x32_bf16 v[40:43], v[172:175], v[212:215], v[40:43]\n"
        "ds_write_b128 v20, v[24:27] offset:28928\n"
        "s_mov_b64 exec, 1\n"
        "ds_add_u32 v16, v5 offset:12\n"
        "s_mov_b64 exec, -1\n"
        "v_mfma_f32_16x16x32_bf16 v[40:43], v[176:179], v[208:211], v[40:43]\n"
        "v_mfma_f32_16x16x32_bf16 v[40:43], v[180:183], v[204:207], v[40:43]\n"
        "v_mfma_f32_16x16x32_bf16 v[40:43], v[184:187], v[200:203], v[40:43]\n"
        "v_mfma_f32_16x16x32_bf16 v[36:39], v[168:171], v[212:215], v[36:39]\n"
        "v_mfma_f32_16x16x32_bf16 v[36:39], v[172:175], v[208:211], v[36:39]\n"
        "v_mfma_f32_16x16x32_bf16 v[36:39], v[176:179], v[204:207], v[36:39]\n"
        "v_mfma_f32_16x16x32_bf16 v[36:39], v[180:183], v[200:203], v[36:39]\n"
        "v_mfma_f32_16x16x32_bf16 v[32:35], v[168:171], v[208:211], v[32:35]\n"
        "v_mfma_f32_16x16x32_bf16 v[32:35], v[172:175], v[204:207], v[32:35]\n"
        "v_mfma_f32_16x16x32_bf16 v[32:35], v[176:179], v[200:203], v[32:35]\n"
        "s_setprio 3\n"
        "s_waitcnt vmcnt(0)\n"
        "v_cmp_ne_u64 s[42:43], s[62:63], v[14:15]\n"
        "s_and_b32 s33, s42, s34\n"
        "s_cbranch_scc1 .Lsl_31_%=\n"
        "buffer_load_dwordx4 v[192:195], v12, s[16:19], s64 offen sc1\n"
        "buffer_load_dwordx4 v[196:199], v12, s[16:19], s64 offen offset:256 sc1\n"
        "buffer_load_dwordx4 v[200:203], v12, s[16:19], s65 offen sc1\n"
        "buffer_load_dwordx4 v[204:207], v12, s[16:19], s65 offen offset:256 sc1\n"
        "buffer_load_dwordx4 v[208:211], v12, s[16:19], s66 offen sc1\n"
        "buffer_load_dwordx4 v[212:215], v12, s[16:19], s66 offen offset:256 sc1\n"
        "s_branch .Ljn_32_%=\n"
        ".Lsl_31_%=:\n"
        "s_and_b64 exec, s[42:43], s[38:39]\n"
        "buffer_load_dwordx2 v[14:15], v255, s[16:19], s32 offen sc1\n"
        "s_mov_b64 exec, -1\n"
        ".Ljn_32_%=:\n"
        "s_add_u32 s40, s81, 4096\n"
        "s_add_u32 s41, s81, 2048\n"
        ".Llw_33_%=:\n"
        "ds_read_b32 v13, v16 offset:12\n"
        "s_waitcnt lgkmcnt(0)\n"
        "v_readfirstlane_b32 s75, v13\n"
        "s_cmp_ge_u32 s75, 2\n"
        "s_cbranch_scc1 .Llwd_34_%=\n"
        "s_add_u32 s61, s61, 1\n"
        "s_cmp_lt_u32 s61, 0x100000\n"
        "s_cbranch_scc1 .Llw_33_%=\n"
        ".Llwd_34_%=:\n"
        "ds_read_b128 v[172:175], v21 offset:25856\n"
        "ds_read_b128 v[176:179], v21 offset:26880\n"
        "ds_read_b128 v[180:183], v21 offset:27904\n"
        "ds_read_b128 v[184:187], v21 offset:28928\n"
        "s_mov_b64 exec, 1\n"
        "ds_add_u32 v4, v5 offset:4\n"
        "s_mov_b64 exec, -1\n"
        "s_waitcnt lgkmcnt(4)\n"
        "v_add_f32 v44, v44, v172\n"
        "v_add_f32 v45, v45, v173\n"
        "v_add_f32 v46, v46, v174\n"
        "v_add_f32 v47, v47, v175\n"
        "v_cvt_pk_bf16_f32 v232, v44, v45\n"
        "v_cvt_pk_bf16_f32 v234, v46, v47\n"
        "s_waitcnt lgkmcnt(3)\n"
        "v_add_f32 v40, v40, v176\n"
        "v_add_f32 v41, v41, v177\n"
        "v_add_f32 v42, v42, v178\n"
        "v_add_f32 v43, v43, v179\n"
        "v_cvt_pk_bf16_f32 v236, v40, v41\n"
        "v_cvt_pk_bf16_f32 v238, v42, v43\n"
        "s_waitcnt lgkmcnt(2)\n"
        "v_add_f32 v36, v36, v180\n"
        "v_add_f32 v37, v37, v181\n"
        "v_add_f32 v38, v38, v182\n"
        "v_add_f32 v39, v39, v183\n"
        "v_cvt_pk_bf16_f32 v240, v36, v37\n"
        "v_cvt_pk_bf16_f32 v242, v38, v39\n"
        "s_waitcnt lgkmcnt(1)\n"
        "v_add_f32 v32, v32, v184\n"
        "v_add_f32 v33, v33, v185\n"
        "v_add_f32 v34, v34, v186\n"
        "v_add_f32 v35, v35, v187\n"
        "v_cvt_pk_bf16_f32 v244, v32, v33\n"
        "v_cvt_pk_bf16_f32 v246, v34, v35\n"
        ".Ly_26_%=:\n"
        "s_setprio 0\n"
        "s_and_b32 s33, s42, s34\n"
        "s_cbranch_scc0 .Lfast_%=\n"
        "buffer_store_dwordx4 v[232:235], v19, s[16:19], s40 offen offset:1024 sc1\n"
        "buffer_store_dwordx4 v[236:239], v19, s[16:19], s40 offen offset:0 sc1\n"
        "buffer_store_dwordx4 v[240:243], v19, s[16:19], s41 offen offset:1024 sc1\n"
        "buffer_store_dwordx4 v[244:247], v19, s[16:19], s41 offen offset:0 sc1\n"
        "s_mov_b32 s40, 2130706432\n"
        "s_mov_b32 s41, 2130706432\n"
        "s_waitcnt vmcnt(4)\n"
        "v_cmp_ne_u64 s[42:43], s[62:63], v[14:15]\n"
        "s_and_b32 s33, s42, s34\n"
        "s_cbranch_scc0 .Lcpd_36_%=\n"
        ".Lcp_35_%=:\n"
        "s_and_b64 exec, s[42:43], s[38:39]\n"
        "buffer_load_dwordx2 v[14:15], v255, s[16:19], s32 offen sc1\n"
        "s_waitcnt vmcnt(0)\n"
        "v_cmp_ne_u64 s[42:43], s[62:63], v[14:15]\n"
        "s_mov_b64 exec, -1\n"
        "s_and_b32 s33, s42, s34\n"
        "s_cbranch_scc0 .Lcpd_36_%=\n"
        "s_sleep 1\n"
        "s_add_u32 s61, s61, 16\n"
        "s_cmp_lt_u32 s61, 0x100000\n"
        "s_cbranch_scc1 .Lcp_35_%=\n"
        ".Lcpd_36_%=:\n"
        "buffer_load_dwordx4 v[192:195], v12, s[16:19], s64 offen sc1\n"
        "buffer_load_dwordx4 v[196:199], v12, s[16:19], s64 offen offset:256 sc1\n"
        "buffer_load_dwordx4 v[200:203], v12, s[16:19], s65 offen sc1\n"
        "buffer_load_dwordx4 v[204:207], v12, s[16:19], s65 offen offset:256 sc1\n"
        "buffer_load_dwordx4 v[208:211], v12, s[16:19], s66 offen sc1\n"
        "buffer_load_dwordx4 v[212:215], v12, s[16:19], s66 offen offset:256 sc1\n"
        ".Lfast_%=:\n"
        "buffer_store_dwordx4 v[232:235], v19, s[16:19], s40 offen offset:1024 sc1\n"
        "buffer_store_dwordx4 v[236:239], v19, s[16:19], s40 offen offset:0 sc1\n"
        "buffer_store_dwordx4 v[240:243], v19, s[16:19], s41 offen offset:1024 sc1\n"
        "buffer_store_dwordx4 v[244:247], v19, s[16:19], s41 offen offset:0 sc1\n"
        "s_mul_i32 s81, s82, 45056\n"
        "s_lshl_b32 s75, s83, 13\n"
        "s_add_u32 s81, s81, s75\n"
        "s_add_u32 s81, s81, 45056\n"
        ".Llw_37_%=:\n"
        "ds_read_b32 v13, v4 offset:8\n"
        "s_waitcnt lgkmcnt(0)\n"
        "v_readfirstlane_b32 s75, v13\n"
        "s_cmp_ge_u32 s75, 4\n"
        "s_cbranch_scc1 .Llwd_38_%=\n"
        "s_add_u32 s61, s61, 1\n"
        "s_cmp_lt_u32 s61, 0x100000\n"
        "s_cbranch_scc1 .Llw_37_%=\n"
        ".Llwd_38_%=:\n"
        "s_lshl2_add_u32 s78, s47, s46\n"
        "s_lshl3_add_u32 s75, s54, s78\n"
        "s_mul_i32 s83, s75, 66560\n"
        "s_lshl_b32 s76, s53, 8\n"
        "s_add_u32 s83, s83, s76\n"
        "s_mul_i32 s76, s50, 0\n"
        "s_sub_u32 s83, s83, s76\n"
        "s_add_u32 s83, s83, 2166784\n"
        "s_lshl2_add_u32 s96, s53, s46\n"
        "s_lshl_b32 s96, s96, 14\n"
        "s_lshl1_add_u32 s76, s54, s47\n"
        "s_lshl_b32 s76, s76, 6\n"
        "s_add_u32 s96, s96, s76\n"
        "v_and_b32 v170, 3, v0\n"
        "v_bfe_u32 v189, v0, 2, 1\n"
        "v_lshrrev_b32 v178, 3, v0\n"
        "v_mul_u32_u24 v179, 8320, v178\n"
        "v_lshl_add_u32 v179, v170, 4, v179\n"
        "v_lshl_add_u32 v171, v189, 7, v179\n"
        "v_lshl_add_u32 v178, v178, 2, v170\n"
        "v_lshl_add_u32 v178, v178, 1, v189\n"
        "v_lshlrev_b32 v178, 4, v178\n"
        "s_and_b32 s76, s46, 1\n"
        "s_lshl_b32 s77, s76, 1\n"
        "v_and_b32 v179, 1, v2\n"
        "v_or_b32 v172, s77, v179\n"
        "v_lshlrev_b32 v172, 5, v172\n"
        "v_xor_b32 v172, v172, v178\n"
        "s_lshl_b32 s77, s78, 10\n"
        "v_add_u32 v172, s77, v172\n"
        "v_add_u32 v176, s81, v3\n"
        "s_lshr_b32 s77, s46, 1\n"
        "s_lshl1_add_u32 s77, s77, s47\n"
        "s_lshl2_add_u32 s77, s77, s76\n"
        "s_lshl_b32 s77, s77, 10\n"
        "v_lshrrev_b32 v178, 1, v2\n"
        "v_lshlrev_b32 v178, 11, v178\n"
        "v_lshl_add_u32 v178, v179, 9, v178\n"
        "v_lshl_add_u32 v178, v1, 4, v178\n"
        "v_add_u32 v173, s77, v178\n"
        "v_add_u32 v177, 45056, v173\n"
        "s_lshl_b32 s76, s46, 5\n"
        "s_lshl_b32 s77, s47, 12\n"
        "v_and_b32 v178, 0xfff, v8\n"
        "v_xor_b32 v178, s76, v178\n"
        "v_add_u32 v174, s77, v178\n"
        "v_lshlrev_b32 v178, 10, v1\n"
        "v_lshl_add_u32 v175, v2, 4, v178\n"
        "v_lshlrev_b32 v188, 3, v1\n"
        "v_and_b32 v178, 0x3c, v0\n"
        "v_lshl_add_u32 v189, v170, 6, v178\n"
        "v_lshlrev_b32 v178, 8, v178\n"
        "v_lshl_add_u32 v175, v170, 4, v178\n"
        "s_and_b32 s33, s42, s36\n"
        "s_cbranch_scc0 .Lcpd_40_%=\n"
        ".Lcp_39_%=:\n"
        "s_and_b64 exec, s[42:43], s[38:39]\n"
        "buffer_load_dwordx2 v[14:15], v255, s[16:19], s32 offen sc1\n"
        "s_waitcnt vmcnt(0)\n"
        "v_cmp_ne_u64 s[42:43], s[62:63], v[14:15]\n"
        "s_mov_b64 exec, -1\n"
        "s_and_b32 s33, s42, s36\n"
        "s_cbranch_scc0 .Lcpd_40_%=\n"
        "s_sleep 1\n"
        "s_add_u32 s61, s61, 16\n"
        "s_cmp_lt_u32 s61, 0x100000\n"
        "s_cbranch_scc1 .Lcp_39_%=\n"
        ".Lcpd_40_%=:\n"
        "s_waitcnt vmcnt(8)\n"
        "buffer_load_dwordx4 v[216:219], v12, s[16:19], s67 offen sc1\n"
        "buffer_load_dwordx4 v[220:223], v12, s[16:19], s67 offen offset:256 sc1\n"
        "s_waitcnt lgkmcnt(0)\n"
        "v_mfma_f32_16x16x32_bf16 v[24:27], v[56:59], v[192:195], 0\n"
        "v_mfma_f32_16x16x32_bf16 v[28:31], v[56:59], v[196:199], 0\n"
        "v_mfma_f32_16x16x32_bf16 v[36:39], v[60:63], v[196:199], 0\n"
        "v_mfma_f32_16x16x32_bf16 v[32:35], v[60:63], v[192:195], 0\n"
        "v_mfma_f32_16x16x32_bf16 v[40:43], v[64:67], v[192:195], 0\n"
        "v_mfma_f32_16x16x32_bf16 v[44:47], v[64:67], v[196:199], 0\n"
        "v_mfma_f32_16x16x32_bf16 v[52:55], v[68:71], v[196:199], 0\n"
        "v_mfma_f32_16x16x32_bf16 v[48:51], v[68:71], v[192:195], 0\n"
        "s_waitcnt vmcnt(8)\n"
        "buffer_load_dwordx4 v[192:195], v12, s[16:19], s68 offen sc1\n"
        "buffer_load_dwordx4 v[196:199], v12, s[16:19], s68 offen offset:256 sc1\n"
        "v_mfma_f32_16x16x32_bf16 v[24:27], v[72:75], v[200:203], v[24:27]\n"
        "ds_read_b64_tr_b16 v[56:57], v8 offset:28672\n"
        "v_mfma_f32_16x16x32_bf16 v[28:31], v[72:75], v[204:207], v[28:31]\n"
        "ds_read_b64_tr_b16 v[58:59], v8 offset:29184\n"
        "v_mfma_f32_16x16x32_bf16 v[36:39], v[76:79], v[204:207], v[36:39]\n"
        "ds_read_b64_tr_b16 v[60:61], v9 offset:28672\n"
        "v_mfma_f32_16x16x32_bf16 v[32:35], v[76:79], v[200:203], v[32:35]\n"
        "ds_read_b64_tr_b16 v[62:63], v9 offset:29184\n"
        "v_mfma_f32_16x16x32_bf16 v[40:43], v[80:83], v[200:203], v[40:43]\n"
        "ds_read_b64_tr_b16 v[64:65], v10 offset:28672\n"
        "v_mfma_f32_16x16x32_bf16 v[44:47], v[80:83], v[204:207], v[44:47]\n"
        "ds_read_b64_tr_b16 v[66:67], v10 offset:29184\n"
        "v_mfma_f32_16x16x32_bf16 v[52:55], v[84:87], v[204:207], v[52:55]\n"
        "ds_read_b64_tr_b16 v[68:69], v11 offset:28672\n"
        "v_mfma_f32_16x16x32_bf16 v[48:51], v[84:87], v[200:203], v[48:51]\n"
        "ds_read_b64_tr_b16 v[70:71], v11 offset:29184\n"
        "s_waitcnt vmcnt(8)\n"
        "buffer_load_dwordx4 v[200:203], v12, s[16:19], s69 offen sc1\n"
        "buffer_load_dwordx4 v[204:207], v12, s[16:19], s69 offen offset:256 sc1\n"
        "s_and_b32 s33, s42, s38\n"
        "s_cbranch_scc0 .Lcpd_42_%=\n"
        ".Lcp_41_%=:\n"
        "s_and_b64 exec, s[42:43], s[38:39]\n"
        "buffer_load_dwordx2 v[14:15], v255, s[16:19], s32 offen sc1\n"
        "s_waitcnt vmcnt(0)\n"
        "v_cmp_ne_u64 s[42:43], s[62:63], v[14:15]\n"
        "s_mov_b64 exec, -1\n"
        "s_and_b32 s33, s42, s38\n"
        "s_cbranch_scc0 .Lcpd_42_%=\n"
        "s_sleep 1\n"
        "s_add_u32 s61, s61, 16\n"
        "s_cmp_lt_u32 s61, 0x100000\n"
        "s_cbranch_scc1 .Lcp_41_%=\n"
        ".Lcpd_42_%=:\n"
        "v_mfma_f32_16x16x32_bf16 v[24:27], v[88:91], v[208:211], v[24:27]\n"
        "ds_read_b64_tr_b16 v[72:73], v8 offset:32768\n"
        "v_mfma_f32_16x16x32_bf16 v[28:31], v[88:91], v[212:215], v[28:31]\n"
        "ds_read_b64_tr_b16 v[74:75], v8 offset:33280\n"
        "v_mfma_f32_16x16x32_bf16 v[36:39], v[92:95], v[212:215], v[36:39]\n"
        "ds_read_b64_tr_b16 v[76:77], v9 offset:32768\n"
        "v_mfma_f32_16x16x32_bf16 v[32:35], v[92:95], v[208:211], v[32:35]\n"
        "ds_read_b64_tr_b16 v[78:79], v9 offset:33280\n"
        "v_mfma_f32_16x16x32_bf16 v[40:43], v[96:99], v[208:211], v[40:43]\n"
        "ds_read_b64_tr_b16 v[80:81], v10 offset:32768\n"
        "v_mfma_f32_16x16x32_bf16 v[44:47], v[96:99], v[212:215], v[44:47]\n"
        "ds_read_b64_tr_b16 v[82:83], v10 offset:33280\n"
        "v_mfma_f32_16x16x32_bf16 v[52:55], v[100:103], v[212:215], v[52:55]\n"
        "ds_read_b64_tr_b16 v[84:85], v11 offset:32768\n"
        "v_mfma_f32_16x16x32_bf16 v[48:51], v[100:103], v[208:211], v[48:51]\n"
        "ds_read_b64_tr_b16 v[86:87], v11 offset:33280\n"
        "s_waitcnt vmcnt(4)\n"
        "buffer_load_dwordx4 v[208:211], v12, s[16:19], s70 offen sc1\n"
        "buffer_load_dwordx4 v[212:215], v12, s[16:19], s70 offen offset:256 sc1\n"
        "v_mfma_f32_16x16x32_bf16 v[24:27], v[104:107], v[216:219], v[24:27]\n"
        "ds_read_b64_tr_b16 v[88:89], v8 offset:36864\n"
        "v_mfma_f32_16x16x32_bf16 v[28:31], v[104:107], v[220:223], v[28:31]\n"
        "ds_read_b64_tr_b16 v[90:91], v8 offset:37376\n"
        "v_mfma_f32_16x16x32_bf16 v[36:39], v[108:111], v[220:223], v[36:39]\n"
        "ds_read_b64_tr_b16 v[92:93], v9 offset:36864\n"
        "v_mfma_f32_16x16x32_bf16 v[32:35], v[108:111], v[216:219], v[32:35]\n"
        "ds_read_b64_tr_b16 v[94:95], v9 offset:37376\n"
        "v_mfma_f32_16x16x32_bf16 v[40:43], v[112:115], v[216:219], v[40:43]\n"
        "ds_read_b64_tr_b16 v[96:97], v10 offset:36864\n"
        "v_mfma_f32_16x16x32_bf16 v[44:47], v[112:115], v[220:223], v[44:47]\n"
        "ds_read_b64_tr_b16 v[98:99], v10 offset:37376\n"
        "v_mfma_f32_16x16x32_bf16 v[52:55], v[116:119], v[220:223], v[52:55]\n"
        "ds_read_b64_tr_b16 v[100:101], v11 offset:36864\n"
        "v_mfma_f32_16x16x32_bf16 v[48:51], v[116:119], v[216:219], v[48:51]\n"
        "ds_read_b64_tr_b16 v[102:103], v11 offset:37376\n"
        "s_waitcnt vmcnt(4)\n"
        "buffer_load_dwordx4 v[216:219], v12, s[16:19], s71 offen sc1\n"
        "buffer_load_dwordx4 v[220:223], v12, s[16:19], s71 offen offset:256 sc1\n"
        "v_mfma_f32_16x16x32_bf16 v[24:27], v[120:123], v[192:195], v[24:27]\n"
        "ds_read_b64_tr_b16 v[104:105], v8 offset:40960\n"
        "v_mfma_f32_16x16x32_bf16 v[28:31], v[120:123], v[196:199], v[28:31]\n"
        "ds_read_b64_tr_b16 v[106:107], v8 offset:41472\n"
        "v_mfma_f32_16x16x32_bf16 v[36:39], v[124:127], v[196:199], v[36:39]\n"
        "ds_read_b64_tr_b16 v[108:109], v9 offset:40960\n"
        "v_mfma_f32_16x16x32_bf16 v[32:35], v[124:127], v[192:195], v[32:35]\n"
        "ds_read_b64_tr_b16 v[110:111], v9 offset:41472\n"
        "v_mfma_f32_16x16x32_bf16 v[40:43], v[128:131], v[192:195], v[40:43]\n"
        "ds_read_b64_tr_b16 v[112:113], v10 offset:40960\n"
        "v_mfma_f32_16x16x32_bf16 v[44:47], v[128:131], v[196:199], v[44:47]\n"
        "ds_read_b64_tr_b16 v[114:115], v10 offset:41472\n"
        "v_mfma_f32_16x16x32_bf16 v[52:55], v[132:135], v[196:199], v[52:55]\n"
        "ds_read_b64_tr_b16 v[116:117], v11 offset:40960\n"
        "v_mfma_f32_16x16x32_bf16 v[48:51], v[132:135], v[192:195], v[48:51]\n"
        "ds_read_b64_tr_b16 v[118:119], v11 offset:41472\n"
        "s_waitcnt vmcnt(4)\n"
        "buffer_load_dwordx4 v[192:195], v12, s[16:19], s72 offen sc1\n"
        "buffer_load_dwordx4 v[196:199], v12, s[16:19], s72 offen offset:256 sc1\n"
        "v_mfma_f32_16x16x32_bf16 v[24:27], v[136:139], v[200:203], v[24:27]\n"
        "v_mfma_f32_16x16x32_bf16 v[28:31], v[136:139], v[204:207], v[28:31]\n"
        "v_mfma_f32_16x16x32_bf16 v[36:39], v[140:143], v[204:207], v[36:39]\n"
        "v_mfma_f32_16x16x32_bf16 v[32:35], v[140:143], v[200:203], v[32:35]\n"
        "v_mfma_f32_16x16x32_bf16 v[40:43], v[144:147], v[200:203], v[40:43]\n"
        "v_mfma_f32_16x16x32_bf16 v[44:47], v[144:147], v[204:207], v[44:47]\n"
        "v_mfma_f32_16x16x32_bf16 v[52:55], v[148:151], v[204:207], v[52:55]\n"
        "v_mfma_f32_16x16x32_bf16 v[48:51], v[148:151], v[200:203], v[48:51]\n"
        "s_waitcnt vmcnt(4)\n"
        "buffer_load_dwordx4 v[200:203], v12, s[16:19], s73 offen sc1\n"
        "buffer_load_dwordx4 v[204:207], v12, s[16:19], s73 offen offset:256 sc1\n"
        "v_mfma_f32_16x16x32_bf16 v[24:27], v[152:155], v[208:211], v[24:27]\n"
        "v_mfma_f32_16x16x32_bf16 v[28:31], v[152:155], v[212:215], v[28:31]\n"
        "v_mfma_f32_16x16x32_bf16 v[36:39], v[156:159], v[212:215], v[36:39]\n"
        "v_mfma_f32_16x16x32_bf16 v[32:35], v[156:159], v[208:211], v[32:35]\n"
        "v_mfma_f32_16x16x32_bf16 v[40:43], v[160:163], v[208:211], v[40:43]\n"
        "v_mfma_f32_16x16x32_bf16 v[44:47], v[160:163], v[212:215], v[44:47]\n"
        "v_mfma_f32_16x16x32_bf16 v[52:55], v[164:167], v[212:215], v[52:55]\n"
        "v_mfma_f32_16x16x32_bf16 v[48:51], v[164:167], v[208:211], v[48:51]\n"
        "s_waitcnt vmcnt(4)\n"
        "buffer_load_dwordx4 v[208:211], v12, s[16:19], s74 offen sc1\n"
        "buffer_load_dwordx4 v[212:215], v12, s[16:19], s74 offen offset:256 sc1\n"
        "s_waitcnt lgkmcnt(0)\n"
        "v_mfma_f32_16x16x32_bf16 v[24:27], v[56:59], v[216:219], v[24:27]\n"
        "v_mfma_f32_16x16x32_bf16 v[28:31], v[56:59], v[220:223], v[28:31]\n"
        "v_mfma_f32_16x16x32_bf16 v[36:39], v[60:63], v[220:223], v[36:39]\n"
        "v_mfma_f32_16x16x32_bf16 v[32:35], v[60:63], v[216:219], v[32:35]\n"
        "v_mfma_f32_16x16x32_bf16 v[40:43], v[64:67], v[216:219], v[40:43]\n"
        "v_mfma_f32_16x16x32_bf16 v[44:47], v[64:67], v[220:223], v[44:47]\n"
        "v_mfma_f32_16x16x32_bf16 v[52:55], v[68:71], v[220:223], v[52:55]\n"
        "v_mfma_f32_16x16x32_bf16 v[48:51], v[68:71], v[216:219], v[48:51]\n"
        "s_waitcnt vmcnt(4)\n"
        "v_mfma_f32_16x16x32_bf16 v[24:27], v[72:75], v[192:195], v[24:27]\n"
        "v_mfma_f32_16x16x32_bf16 v[28:31], v[72:75], v[196:199], v[28:31]\n"
        "v_mfma_f32_16x16x32_bf16 v[36:39], v[76:79], v[196:199], v[36:39]\n"
        "v_mfma_f32_16x16x32_bf16 v[32:35], v[76:79], v[192:195], v[32:35]\n"
        "v_mfma_f32_16x16x32_bf16 v[40:43], v[80:83], v[192:195], v[40:43]\n"
        "v_mfma_f32_16x16x32_bf16 v[44:47], v[80:83], v[196:199], v[44:47]\n"
        "v_mfma_f32_16x16x32_bf16 v[52:55], v[84:87], v[196:199], v[52:55]\n"
        "v_mfma_f32_16x16x32_bf16 v[48:51], v[84:87], v[192:195], v[48:51]\n"
        "s_waitcnt vmcnt(2)\n"
        "buffer_load_dwordx4 v[180:183], v171, s[16:19], s83 offen sc1\n"
        "buffer_load_dwordx4 v[184:187], v171, s[16:19], s83 offen offset:64 sc1\n"
        "buffer_load_dwordx2 v[168:169], v188, s[16:19], s49 offen sc1\n"
        "v_mfma_f32_16x16x32_bf16 v[24:27], v[88:91], v[200:203], v[24:27]\n"
        "v_mfma_f32_16x16x32_bf16 v[28:31], v[88:91], v[204:207], v[28:31]\n"
        "v_mfma_f32_16x16x32_bf16 v[36:39], v[92:95], v[204:207], v[36:39]\n"
        "v_mfma_f32_16x16x32_bf16 v[32:35], v[92:95], v[200:203], v[32:35]\n"
        "v_mfma_f32_16x16x32_bf16 v[40:43], v[96:99], v[200:203], v[40:43]\n"
        "v_mfma_f32_16x16x32_bf16 v[44:47], v[96:99], v[204:207], v[44:47]\n"
        "v_mfma_f32_16x16x32_bf16 v[52:55], v[100:103], v[204:207], v[52:55]\n"
        "v_mfma_f32_16x16x32_bf16 v[48:51], v[100:103], v[200:203], v[48:51]\n"
        "s_cmp_ge_u32 s78, 6\n"
        "s_cbranch_scc1 .Lg2e_%=\n"
        "s_waitcnt vmcnt(3)\n"
        "v_mfma_f32_16x16x32_bf16 v[24:27], v[104:107], v[208:211], v[24:27]\n"
        "v_mfma_f32_16x16x32_bf16 v[28:31], v[104:107], v[212:215], v[28:31]\n"
        "v_mfma_f32_16x16x32_bf16 v[36:39], v[108:111], v[212:215], v[36:39]\n"
        "v_mfma_f32_16x16x32_bf16 v[32:35], v[108:111], v[208:211], v[32:35]\n"
        "v_mfma_f32_16x16x32_bf16 v[40:43], v[112:115], v[208:211], v[40:43]\n"
        "v_mfma_f32_16x16x32_bf16 v[44:47], v[112:115], v[212:215], v[44:47]\n"
        "v_mfma_f32_16x16x32_bf16 v[52:55], v[116:119], v[212:215], v[52:55]\n"
        "v_mfma_f32_16x16x32_bf16 v[48:51], v[116:119], v[208:211], v[48:51]\n"
        ".Lg2e_%=:\n"
        ".Llw_43_%=:\n"
        "ds_read_b32 v13, v4 offset:4\n"
        "s_waitcnt lgkmcnt(0)\n"
        "v_readfirstlane_b32 s75, v13\n"
        "s_cmp_ge_u32 s75, 8\n"
        "s_cbranch_scc1 .Llwd_44_%=\n"
        "s_add_u32 s61, s61, 1\n"
        "s_cmp_lt_u32 s61, 0x100000\n"
        "s_cbranch_scc1 .Llw_43_%=\n"
        ".Llwd_44_%=:\n"
        "s_nop 1\n"
        "ds_write_b128 v176, v[24:27] offset:0\n"
        "ds_write_b128 v176, v[28:31] offset:1024\n"
        "ds_write_b128 v176, v[32:35] offset:2048\n"
        "ds_write_b128 v176, v[36:39] offset:3072\n"
        "ds_write_b128 v176, v[40:43] offset:4096\n"
        "ds_write_b128 v176, v[44:47] offset:5120\n"
        "ds_write_b128 v176, v[48:51] offset:6144\n"
        "ds_write_b128 v176, v[52:55] offset:7168\n"
        ".Lar_%=:\n"
        "s_waitcnt vmcnt(0)\n"
        "v_xor_b32 v181, s62, v181\n"
        "v_xor_b32 v183, s62, v183\n"
        "v_xor_b32 v185, s62, v185\n"
        "v_xor_b32 v187, s62, v187\n"
        "v_xor_b32 v169, s62, v169\n"
        "v_or3_b32 v181, v181, v183, v185\n"
        "v_or3_b32 v187, v181, v187, v169\n"
        "v_cmp_ne_u32 vcc, 0, v187\n"
        "v_mov_b32 v181, v182\n"
        "v_mov_b32 v182, v184\n"
        "v_mov_b32 v183, v186\n"
        "s_cbranch_vccz .Lad_%=\n"
        "s_add_u32 s61, s61, 16\n"
        "s_cmp_lt_u32 s61, 0x100000\n"
        "s_cbranch_scc0 .Lad_%=\n"
        "buffer_load_dwordx4 v[180:183], v171, s[16:19], s83 offen sc1\n"
        "buffer_load_dwordx4 v[184:187], v171, s[16:19], s83 offen offset:64 sc1\n"
        "buffer_load_dwordx2 v[168:169], v188, s[16:19], s49 offen sc1\n"
        "s_branch .Lar_%=\n"
        ".Lad_%=:\n"
        "ds_write_b128 v172, v[180:183] offset:33792\n"
        "s_waitcnt lgkmcnt(0)\n"
        "s_barrier\n"
        "ds_read_b64_tr_b16 v[56:57], v174 offset:33792\n"
        "ds_read_b64_tr_b16 v[58:59], v174 offset:34304\n"
        "ds_read_b128 v[72:75], v173 offset:1024\n"
        "ds_read_b128 v[76:79], v173 offset:1280\n"
        "ds_read_b128 v[80:83], v173 offset:17408\n"
        "ds_read_b128 v[84:87], v173 offset:17664\n"
        "ds_read_b128 v[88:91], v177 offset:0\n"
        "ds_read_b128 v[92:95], v177 offset:256\n"
        "ds_read_b128 v[96:99], v177 offset:45056\n"
        "ds_read_b128 v[100:103], v177 offset:45312\n"
        "v_sub_f32 v169, 1.0, v168\n"
        "v_cvt_pk_bf16_f32 v169, v169, v169\n"
        "v_lshlrev_b32 v169, 16, v169\n"
        "s_waitcnt lgkmcnt(8)\n"
        "v_lshlrev_b32 v60, 16, v56\n"
        "v_and_b32 v61, 0xffff0000, v56\n"
        "v_lshlrev_b32 v62, 16, v57\n"
        "v_and_b32 v63, 0xffff0000, v57\n"
        "v_lshlrev_b32 v64, 16, v58\n"
        "v_and_b32 v65, 0xffff0000, v58\n"
        "v_lshlrev_b32 v66, 16, v59\n"
        "v_and_b32 v67, 0xffff0000, v59\n"
        "v_pk_mul_f32 v[60:61], v[60:61], v[168:169] op_sel_hi:[1,0]\n"
        "v_pk_mul_f32 v[62:63], v[62:63], v[168:169] op_sel_hi:[1,0]\n"
        "v_pk_mul_f32 v[64:65], v[64:65], v[168:169] op_sel_hi:[1,0]\n"
        "v_pk_mul_f32 v[66:67], v[66:67], v[168:169] op_sel_hi:[1,0]\n"
        "s_waitcnt lgkmcnt(4)\n"
        "v_pk_add_f32 v[72:73], v[72:73], v[80:81]\n"
        "v_pk_add_f32 v[74:75], v[74:75], v[82:83]\n"
        "v_pk_add_f32 v[76:77], v[76:77], v[84:85]\n"
        "v_pk_add_f32 v[78:79], v[78:79], v[86:87]\n"
        "s_waitcnt lgkmcnt(2)\n"
        "v_pk_add_f32 v[72:73], v[72:73], v[88:89]\n"
        "v_pk_add_f32 v[74:75], v[74:75], v[90:91]\n"
        "v_pk_add_f32 v[76:77], v[76:77], v[92:93]\n"
        "v_pk_add_f32 v[78:79], v[78:79], v[94:95]\n"
        "s_waitcnt lgkmcnt(0)\n"
        "v_pk_add_f32 v[72:73], v[72:73], v[96:97]\n"
        "v_pk_add_f32 v[74:75], v[74:75], v[98:99]\n"
        "v_pk_add_f32 v[76:77], v[76:77], v[100:101]\n"
        "v_pk_add_f32 v[78:79], v[78:79], v[102:103]\n"
        "v_pk_fma_f32 v[60:61], v[72:73], v[168:169], v[60:61] op_sel:[0,1,0]\n"
        "v_pk_fma_f32 v[62:63], v[74:75], v[168:169], v[62:63] op_sel:[0,1,0]\n"
        "v_pk_fma_f32 v[64:65], v[76:77], v[168:169], v[64:65] op_sel:[0,1,0]\n"
        "v_pk_fma_f32 v[66:67], v[78:79], v[168:169], v[66:67] op_sel:[0,1,0]\n"
        "v_cvt_pk_bf16_f32 v56, v60, v61\n"
        "v_cvt_pk_bf16_f32 v57, v62, v63\n"
        "v_cvt_pk_bf16_f32 v58, v64, v65\n"
        "v_cvt_pk_bf16_f32 v59, v66, v67\n"
        "ds_bpermute_b32 v56, v189, v56\n"
        "ds_bpermute_b32 v57, v189, v57\n"
        "ds_bpermute_b32 v58, v189, v58\n"
        "ds_bpermute_b32 v59, v189, v59\n"
        "s_waitcnt lgkmcnt(0)\n"
        "buffer_store_dwordx4 v[56:59], v175, s[24:27], s96 offen sc1\n"
        "s_endpgm\n"
        ".Lend_%=:\n"
        :: "s"(__builtin_amdgcn_kernarg_segment_ptr()), "s"(blockIdx.x), "v"(t)
        : "v1", "v2", "v3", "v4", "v5", "v6", "v7", "v8", "v9", "v10", "v11", "v12", "v13", "v14", "v15", "v16", "v17", "v18", "v19", "v20", "v21", "v22", "v23", "v24", "v25", "v26", "v27", "v28", "v29", "v30", "v31", "v32", "v33", "v34", "v35", "v36", "v37", "v38", "v39", "v40", "v41", "v42", "v43", "v44", "v45", "v46", "v47", "v48", "v49", "v50", "v51", "v52", "v53", "v54", "v55", "v56", "v57", "v58", "v59", "v60", "v61", "v62", "v63", "v64", "v65", "v66", "v67", "v68", "v69", "v70", "v71", "v72", "v73", "v74", "v75", "v76", "v77", "v78", "v79", "v80", "v81", "v82", "v83", "v84", "v85", "v86", "v87", "v88", "v89", "v90", "v91", "v92", "v93", "v94", "v95", "v96", "v97", "v98", "v99", "v100", "v101", "v102", "v103", "v104", "v105", "v106", "v107", "v108", "v109", "v110", "v111", "v112", "v113", "v114", "v115", "v116", "v117", "v118", "v119", "v120", "v121", "v122", "v123", "v124", "v125", "v126", "v127", "v128", "v129", "v130", "v131", "v132", "v133", "v134", "v135", "v136", "v137", "v138", "v139", "v140", "v141", "v142", "v143", "v144", "v145", "v146", "v147", "v148", "v149", "v150", "v151", "v152", "v153", "v154", "v155", "v156", "v157", "v158", "v159", "v160", "v161", "v162", "v163", "v164", "v165", "v166", "v167", "v168", "v169", "v170", "v171", "v172", "v173", "v174", "v175", "v176", "v177", "v178", "v179", "v180", "v181", "v182", "v183", "v184", "v185", "v186", "v187", "v188", "v189", "v190", "v191", "v192", "v193", "v194", "v195", "v196", "v197", "v198", "v199", "v200", "v201", "v202", "v203", "v204", "v205", "v206", "v207", "v208", "v209", "v210", "v211", "v212", "v213", "v214", "v215", "v216", "v217", "v218", "v219", "v220", "v221", "v222", "v223", "v224", "v225", "v226", "v227", "v228", "v229", "v230", "v231", "v232", "v233", "v234", "v235", "v236", "v237", "v238", "v239", "v240", "v241", "v242", "v243", "v244", "v245", "v246", "v247", "v248", "v249", "v250", "v251", "v252", "v253", "v254", "v255", "s3", "s4", "s5", "s6", "s7", "s8", "s9", "s10", "s11", "s12", "s13", "s14", "s15", "s16", "s17", "s18", "s19", "s20", "s21", "s22", "s23", "s24", "s25", "s26", "s27", "s28", "s29", "s30", "s31", "s32", "s33", "s34", "s35", "s36", "s37", "s38", "s39", "s40", "s41", "s42", "s43", "s44", "s45", "s46", "s47", "s48", "s49", "s50", "s51", "s52", "s53", "s54", "s55", "s56", "s57", "s58", "s59", "s60", "s61", "s62", "s63", "s64", "s65", "s66", "s67", "s68", "s69", "s70", "s71", "s72", "s73", "s74", "s75", "s76", "s77", "s78", "s79", "s80", "s81", "s82", "s83", "s84", "s85", "s86", "s87", "s88", "s89", "s90", "s91", "s92", "s93", "s94", "s95", "s96", "s97", "vcc", "memory");
}

extern "C" void kernel_launch(void* const* d_in, const int* in_sizes, int n_in,
                              void* d_out, int out_size, void* d_ws, size_t ws_size,
                              hipStream_t stream) {
    const unsigned F = 0x00020000u;
    Args a;
    a.x = {d_in[0], 2048 * 512 * 2, F};
    a.wg = {d_in[10], 512 * 1376 * 2, F};
    a.wu = {d_in[11], 512 * 1376 * 2, F};
    a.ws = {d_ws, 12259328u, F};
    a.wd = {d_in[12], 512 * 1376 * 2, F};
    a.out = {d_out, 2048 * 512 * 2, F};
    for (int i = 0; i < 6; ++i) a.q[i] = d_in[1 + i];
    a.q[6] = d_in[13]; a.q[7] = d_in[7]; a.q[8] = d_in[8];
    (void)hipFuncSetAttribute((const void*)k_fused, hipFuncAttributeMaxDynamicSharedMemorySize, 163840);
    k_fused<<<256, 512, 163840, stream>>>(a);
}
```
